```python
import torch

def _softmax(x, axis=-1):
    return torch.softmax(x, dim=axis)
def _gelu(x):
    return torch.nn.functional.gelu(x, approximate="tanh")
def _silu(x):
    return torch.nn.functional.silu(x)
def _sigmoid(x):
    return torch.sigmoid(x)
def _layernorm(x, axis=-1, eps=1e-5):
    m = x.mean(dim=axis, keepdim=True)
    v = x.var(dim=axis, keepdim=True, unbiased=False)
    return (x - m) / torch.sqrt(v + eps)
def _scatter_add(tgt, idx, src, axis=0):
    return tgt.clone().index_add_(axis, idx, src)
def _segment_sum(src, seg, n):
    out = torch.zeros((n,) + src.shape[1:], dtype=src.dtype)
    return out.index_add_(0, seg, src)
def _pad(x, pw):
    flat = tuple(v for pair in reversed(pw) for v in pair)
    return torch.nn.functional.pad(x, flat)

kernel_name = 'gated_mix__compressed_kv_cla__sliding_block_cached__decode'

def setup_inputs():

    _w = (lambda a: (lambda t: t.to(torch.bfloat16) if t.is_floating_point() else t)(torch.as_tensor(a)))
    def _A_setup():
        B, H, Sq, Sk, D, M = 8, 32, 1, 4096, 128, 512
        Dc = 4 * D
        return {
            "x": _w((lambda *s: torch.randn(*s, dtype=torch.bfloat16) * 0.05)(B, Sq, M)),
            "c_kv": _w((lambda *s: torch.randn(*s, dtype=torch.bfloat16) * 0.4)(B, Sk, Dc)),
            "w_q": _w((lambda *s: torch.randn(*s, dtype=torch.bfloat16) * 6.4)(M, H, D) * (M ** -0.5)),
            "w_uk": _w((lambda *s: torch.randn(*s, dtype=torch.bfloat16) * 6.4)(Dc, H, D) * (Dc ** -0.5)),
            "w_uv": _w((lambda *s: torch.randn(*s, dtype=torch.bfloat16) * 0.4)(Dc, H, D) * (Dc ** -0.5)),
            "w_o": _w((lambda *s: torch.randn(*s, dtype=torch.bfloat16) * 0.4)(H, D, M) * ((H * D) ** -0.5)),
        }
    _A_in = _A_setup()

    def _B_setup():
        B, Sq, D = 8, 1, 512
        Hq, Hkv, Sk, Dh = 32, 8, 4096, 128
        assert Hq % Hkv == 0, (Hq, Hkv)
        return {
            "x": _w((lambda *s: torch.randn(*s, dtype=torch.bfloat16) * 0.05)(B, Sq, D)),
            "k_cache": _w((lambda *s: torch.randn(*s, dtype=torch.bfloat16) * 6.4)(B, Hkv, Sk, Dh)),
            "v_cache": _w((lambda *s: torch.randn(*s, dtype=torch.bfloat16) * 0.05)(B, Hkv, Sk, Dh)),
            "wq": _w((lambda *s: torch.randn(*s, dtype=torch.bfloat16) * 6.4)(D, Hq * Dh) * (D ** -0.5)),
            "wo": _w((lambda *s: torch.randn(*s, dtype=torch.bfloat16) * 0.05)(Hq * Dh, D) * ((Hq * Dh) ** -0.5)),
        }
    _B_in = _B_setup()

    return {
        "A_x": _A_in["x"],
        "A_c_kv": _A_in["c_kv"],
        "A_w_q": _A_in["w_q"],
        "A_w_uk": _A_in["w_uk"],
        "A_w_uv": _A_in["w_uv"],
        "A_w_o": _A_in["w_o"],
        "B_x": _B_in["x"],
        "B_k_cache": _B_in["k_cache"],
        "B_v_cache": _B_in["v_cache"],
        "B_wq": _B_in["wq"],
        "B_wo": _B_in["wo"],
        "gate_w": _w((lambda *s: torch.randn(*s, dtype=torch.bfloat16) * 0.05)(512) * 16.0),
    }

def reference(A_x, A_c_kv, A_w_q, A_w_uk, A_w_uv, A_w_o, B_x, B_k_cache, B_v_cache, B_wq, B_wo, gate_w):
    def _A_ref(x, c_kv, w_q, w_uk, w_uv, w_o):
        B, Sq, M = x.shape[0], x.shape[1], x.shape[2]
        Sk = c_kv.shape[1]
        H, D = w_q.shape[1], w_q.shape[2]
        q = torch.einsum("bqm,mhd->bhqd", x, w_q)
        k = torch.einsum("bkc,chd->bhkd", c_kv, w_uk)
        v = torch.einsum("bkc,chd->bhkd", c_kv, w_uv)
        scores = torch.einsum("bhqd,bhkd->bhqk", q, k) * (D ** -0.5)
        off = Sk - Sq
        qi = torch.arange(Sq)[:, None]
        kj = torch.arange(Sk)[None, :]
        cmask = kj <= (qi + off)
        scores = torch.where(cmask[None, None], scores, -1e9)
        p = _softmax(scores, axis=-1)
        ctx = torch.einsum("bhqk,bhkd->bhqd", p, v)
        return torch.einsum("bhqd,hdm->bqm", ctx, w_o)

    def _B_ref(x, k_cache, v_cache, wq, wo):
        B, Sq, D = x.shape
        Hq, Dh = 32, 128
        Hkv, Sk = k_cache.shape[1], k_cache.shape[2]
        W = min(4096, max(256, Sk // 4))
        rep = Hq // Hkv
        q = (lambda x, *d: x.permute(*d))((x @ wq).reshape(B, Sq, Hq, Dh), 0, 2, 1, 3)
        k = (lambda x, n, axis: torch.repeat_interleave(x, n, dim=axis))(k_cache[:, :, Sk - W:, :], rep, axis=1)
        v = (lambda x, n, axis: torch.repeat_interleave(x, n, dim=axis))(v_cache[:, :, Sk - W:, :], rep, axis=1)
        scores = torch.einsum("bhqd,bhkd->bhqk", q, k) * (Dh ** -0.5)
        p = _softmax(scores, axis=-1)
        o = torch.einsum("bhqk,bhkd->bhqd", p, v)
        return (lambda x, *d: x.permute(*d))(o, 0, 2, 1, 3).reshape(B, Sq, Hq * Dh) @ wo

    x = A_x
    g = _sigmoid((x * gate_w).sum(-1, keepdims=True))
    a = _A_ref(x, A_c_kv, A_w_q, A_w_uk, A_w_uv, A_w_o)
    b = _B_ref(x, B_k_cache, B_v_cache, B_wq, B_wo)
    return g * a + (1 - g) * b
```

```cpp
#include <hip/hip_runtime.h>
#include <hip/hip_bf16.h>
typedef unsigned short u16;
__device__ __forceinline__ float bf2f(u16 v){ return __uint_as_float(((unsigned)v)<<16); }
__device__ __forceinline__ float rbf(float f){ __hip_bfloat16 h = __float2bfloat16(f); return __bfloat162float(h); }

__global__ void k_q(const u16* x, const u16* wqA, const u16* wqB, float* q){
  int col = blockIdx.x*256+threadIdx.x;
  const u16* w = col<4096 ? wqA : wqB; int c = col&4095;
  float acc[8]={0,0,0,0,0,0,0,0};
  for(int m=0;m<512;m++){ float wv=bf2f(w[m*4096+c]);
    for(int b=0;b<8;b++) acc[b]+=bf2f(x[b*512+m])*wv; }
  for(int b=0;b<8;b++) q[((col>>12)*8+b)*4096+c]=rbf(acc[b]);
}
__global__ void k_qlat(const float* q, const u16* wuk, float* qlat){
  int i = blockIdx.x*256+threadIdx.x;
  int h=i>>9, c=i&511;
  float acc[8]={0,0,0,0,0,0,0,0};
  for(int d=0;d<128;d++){ float wv=bf2f(wuk[(c*32+h)*128+d]);
    for(int b=0;b<8;b++) acc[b]+=q[b*4096+h*128+d]*wv; }
  for(int b=0;b<8;b++) qlat[(b*32+h)*512+c]=acc[b];
}
__global__ void k_attnA(const float* qlat, const u16* ckv, float* ctxlat){
  __shared__ float s[4096]; __shared__ float red[256]; __shared__ float ql[512];
  int bh=blockIdx.x, b=bh>>5; int t=threadIdx.x;
  for(int c=t;c<512;c+=256) ql[c]=qlat[bh*512+c];
  __syncthreads();
  float mx=-1e30f;
  for(int k=t;k<4096;k+=256){ float a=0; const u16* r=ckv+((size_t)b*4096+k)*512; for(int c=0;c<512;c++) a+=ql[c]*bf2f(r[c]); a*=0.08838834764831845f; s[k]=a; mx=fmaxf(mx,a);}
  red[t]=mx; __syncthreads(); for(int o=128;o>0;o>>=1){ if(t<o) red[t]=fmaxf(red[t],red[t+o]); __syncthreads(); } mx=red[0]; __syncthreads();
  float sm=0; for(int k=t;k<4096;k+=256){ float e=__expf(s[k]-mx); s[k]=e; sm+=e; }
  red[t]=sm; __syncthreads(); for(int o=128;o>0;o>>=1){ if(t<o) red[t]+=red[t+o]; __syncthreads(); } sm=red[0];
  for(int c=t;c<512;c+=256){ float a=0; for(int k=0;k<4096;k++) a+=s[k]*bf2f(ckv[((size_t)b*4096+k)*512+c]); ctxlat[bh*512+c]=a/sm; }
}
__global__ void k_attnB(const float* q, const u16* kc, const u16* vc, float* o){
  __shared__ float s[1024]; __shared__ float red[256]; __shared__ float ql[128];
  int bh=blockIdx.x, b=bh>>5, h=bh&31, hk=h>>2; int t=threadIdx.x;
  if(t<128) ql[t]=q[(8+b)*4096+h*128+t];
  __syncthreads();
  const u16* K=kc+(((size_t)b*8+hk)*4096+3072)*128; const u16* V=vc+(((size_t)b*8+hk)*4096+3072)*128;
  float mx=-1e30f;
  for(int k=t;k<1024;k+=256){ float a=0; for(int d=0;d<128;d++) a+=ql[d]*bf2f(K[k*128+d]); a*=0.08838834764831845f; s[k]=a; mx=fmaxf(mx,a);}
  red[t]=mx; __syncthreads(); for(int o=128;o>0;o>>=1){ if(t<o) red[t]=fmaxf(red[t],red[t+o]); __syncthreads(); } mx=red[0]; __syncthreads();
  float sm=0; for(int k=t;k<1024;k+=256){ float e=__expf(s[k]-mx); s[k]=e; sm+=e; }
  red[t]=sm; __syncthreads(); for(int o=128;o>0;o>>=1){ if(t<o) red[t]+=red[t+o]; __syncthreads(); } sm=red[0];
  if(t<128){ float a=0; for(int k=0;k<1024;k++) a+=s[k]*bf2f(V[k*128+t]); o[b*4096+h*128+t]=rbf(a/sm); }
}
__global__ void k_uv(const float* ctxlat, const u16* wuv, float* ctx){
  int i=blockIdx.x*256+threadIdx.x;
  int h=i>>7;
  float acc[8]={0,0,0,0,0,0,0,0};
  for(int c=0;c<512;c++){ float wv=bf2f(wuv[c*4096+i]); for(int b=0;b<8;b++) acc[b]+=ctxlat[(b*32+h)*512+c]*wv; }
  for(int b=0;b<8;b++) ctx[b*4096+i]=rbf(acc[b]);
}
__global__ void k_out(const u16* x, const u16* gw, const float* ctx, const float* o, const u16* woA, const u16* woB, __hip_bfloat16* out){
  int b=blockIdx.x>>1; int m=(blockIdx.x&1)*256+threadIdx.x;
  float g=0; for(int i=0;i<512;i++) g+=rbf(bf2f(x[b*512+i])*bf2f(gw[i])); g=rbf(g); g=rbf(1.f/(1.f+__expf(-g)));
  float a=0,bb=0;
  for(int i=0;i<4096;i++){ a+=ctx[b*4096+i]*bf2f(woA[i*512+m]); bb+=o[b*4096+i]*bf2f(woB[i*512+m]); }
  out[b*512+m]=__float2bfloat16(rbf(g*rbf(a))+rbf(rbf(1.f-g)*rbf(bb)));
}
extern "C" void kernel_launch(void* const* d_in, const int* in_sizes, int n_in, void* d_out, int out_size, void* d_ws, size_t ws_size, hipStream_t stream){
  const u16* x=(const u16*)d_in[0]; const u16* ckv=(const u16*)d_in[1]; const u16* wqA=(const u16*)d_in[2];
  const u16* wuk=(const u16*)d_in[3]; const u16* wuv=(const u16*)d_in[4]; const u16* woA=(const u16*)d_in[5];
  const u16* kc=(const u16*)d_in[7]; const u16* vc=(const u16*)d_in[8]; const u16* wqB=(const u16*)d_in[9]; const u16* woB=(const u16*)d_in[10];
  const u16* gw=(const u16*)d_in[11];
  float* ws=(float*)d_ws; float* q=ws; float* qlat=q+2*8*4096; float* ctxlat=qlat+8*32*512; float* o=ctxlat+8*32*512; float* ctx=o+8*4096;
  k_q<<<32,256,0,stream>>>(x,wqA,wqB,q);
  k_qlat<<<64,256,0,stream>>>(q,wuk,qlat);
  k_attnA<<<256,256,0,stream>>>(qlat,ckv,ctxlat);
  k_attnB<<<256,256,0,stream>>>(q,kc,vc,o);
  k_uv<<<16,256,0,stream>>>(ctxlat,wuv,ctx);
  k_out<<<16,256,0,stream>>>(x,gw,ctx,o,woA,woB,(__hip_bfloat16*)d_out);
}
```

```cpp
#pragma clang diagnostic ignored "-Wint-to-pointer-cast"
#include <hip/hip_runtime.h>
#include <hip/hip_bf16.h>
#include <stdint.h>

typedef unsigned int u32;
typedef unsigned long long u64;
typedef u32 u32x4 __attribute__((ext_vector_type(4)));
typedef u32 u32x2 __attribute__((ext_vector_type(2)));
typedef float f32x4 __attribute__((ext_vector_type(4)));
typedef float f32x2 __attribute__((ext_vector_type(2)));
typedef float f32x16 __attribute__((ext_vector_type(16)));
typedef __bf16 bf16x8 __attribute__((ext_vector_type(8)));
typedef __bf16 bf16x4 __attribute__((ext_vector_type(4)));
typedef __bf16 bf16x2 __attribute__((ext_vector_type(2)));
#define LAS __attribute__((address_space(3)))
#define DEV __device__ __forceinline__

#define HMAP (b + 8 * (idx & 3))
#define NCLR 8
#define WA 4
#define NPRE 8
#define NSTEP 4
#define WB 4
#define NTHIN 3

constexpr u32 WS_EPOCH = 0;
constexpr u32 WS_FQ   = 4096;
constexpr u32 WS_FP   = 8192;
constexpr u32 WS_QA   = 16384;
constexpr u32 WS_QB   = WS_QA + 262144;
constexpr u32 WS_QL   = WS_QB + 262144;
constexpr u32 WS_ML   = WS_QL + 262144;
constexpr u32 WS_CT   = WS_ML + 65536;
constexpr u32 WS_BP   = WS_CT + 524288;
constexpr u32 WS_OP   = WS_BP + 262144;
constexpr u32 WS_AP   = WS_OP + 524288;
constexpr u32 WS_BYTES = WS_AP + 4194304;
constexpr u32 WS_DBG = 16u << 20;

constexpr u32 L_X = 131072;
constexpr u32 L_P = L_X, L_MAXB = L_X + 8192, L_SUMB = L_X + 8704;
constexpr u32 L_WUV = 0, L_WOA = 32768, L_WOB = 65536, L_CL = 98304;
constexpr u32 L_WUK = 0, L_WQA = 65536, L_WQB = L_X;
constexpr u32 L_BM = L_X + 16384, L_BML = L_X + 3072, L_QBS = L_X + 2048;
constexpr u32 LDS_BYTES = 163840;
constexpr u32 SENT = 0xFFFFFFFFu;
#define DMA_Q dma_nt
constexpr u32 SPIN_CAP = 1u << 20;
constexpr float SCL = 0.08838834764831845f * 1.4426950408889634f;

struct Params {
  const char *x, *ckv, *wqa, *wuk, *wuv, *woa, *kc, *vc, *wqb, *wob, *gw;
  char *out, *ws;
};

template <int N> DEV void waitv() { asm volatile("s_waitcnt vmcnt(%0)" ::"i"(N) : "memory"); }
DEV void waitl() { asm volatile("s_waitcnt lgkmcnt(0)" ::: "memory"); }
DEV void ldg_nt(u32x4& r, u32 voff, const char* sb) { asm volatile("global_load_dwordx4 %0, %1, %2 nt" : "=v"(r) : "v"(voff), "s"(sb) : "memory"); }
DEV void ldg(u32x4& r, u32 voff, const char* sb) { asm volatile("global_load_dwordx4 %0, %1, %2" : "=v"(r) : "v"(voff), "s"(sb) : "memory"); }
DEV void ldg_sc1(u32x4& r, u32 voff, const char* sb) { asm volatile("global_load_dwordx4 %0, %1, %2 sc1" : "=v"(r) : "v"(voff), "s"(sb) : "memory"); }
DEV void dma_nt(u32 voff, const char* sb, u32 lds) {
  u32 keep;
  asm volatile("s_mov_b32 %0, m0\n\ts_mov_b32 m0, %3\n\ts_nop 0\n\tglobal_load_lds_dwordx4 %1, %2 nt\n\ts_mov_b32 m0, %0" : "=&s"(keep) : "v"(voff), "s"(sb), "s"(lds) : "memory");
}
DEV void dma(u32 voff, const char* sb, u32 lds) {
  u32 keep;
  asm volatile("s_mov_b32 %0, m0\n\ts_mov_b32 m0, %3\n\ts_nop 0\n\tglobal_load_lds_dwordx4 %1, %2\n\ts_mov_b32 m0, %0" : "=&s"(keep) : "v"(voff), "s"(sb), "s"(lds) : "memory");
}
DEV void pin(u32x4& r) { asm volatile("" : "+v"(r)); }
#define CFENCE() asm volatile("" ::: "memory")

typedef __amdgpu_buffer_rsrc_t rsrc_t;
DEV u32x4 wsld(rsrc_t r, u32 off) { return __builtin_amdgcn_raw_buffer_load_b128(r, off, 0, 16); }
DEV u32x2 wsld2(rsrc_t r, u32 off) { return __builtin_amdgcn_raw_buffer_load_b64(r, off, 0, 16); }
DEV void wsst(rsrc_t r, u32 off, u32x4 v) { __builtin_amdgcn_raw_buffer_store_b128(v, r, off, 0, 16); }
DEV void wsst2(rsrc_t r, u32 off, u32x2 v) { __builtin_amdgcn_raw_buffer_store_b64(v, r, off, 0, 16); }
DEV u32 umax3(u32 a, u32 b, u32 c) { return max(max(a, b), c); }
DEV u32 vmax4(u32 m, u32x4 v) { return umax3(umax3(m, v[0], v[1]), v[2], v[3]); }
DEV u32 clampw(u32 v) { return min(v, 0xFFFFFFFEu); }
DEV u32x4 clamp4(u32x4 v) { return u32x4{clampw(v[0]), clampw(v[1]), clampw(v[2]), clampw(v[3])}; }
DEV u32 pk(float a, float b) { return __builtin_bit_cast(u32, __builtin_convertvector(f32x2{a, b}, bf16x2)); }
DEV float lo2f(u32 v) { return __uint_as_float(v << 16); }
DEV float hi2f(u32 v) { return __uint_as_float(v & 0xFFFF0000u); }
DEV float rbf(float f) { return lo2f(pk(f, 0.f)); }
DEV bf16x8 asbf(u32x4 v) { return __builtin_bit_cast(bf16x8, v); }
DEV f32x4 mfma16(u32x4 a, u32x4 b, f32x4 c) { return __builtin_amdgcn_mfma_f32_16x16x32_bf16(asbf(a), asbf(b), c, 0, 0, 0); }
DEV f32x16 mfma32(u32x4 a, u32x4 b, f32x16 c) { return __builtin_amdgcn_mfma_f32_32x32x16_bf16(asbf(a), asbf(b), c, 0, 0, 0); }
DEV u32x4 lds128(u32 a) { return *(LAS u32x4*)a; }
DEV void lds128w(u32 a, u32x4 v) { *(LAS u32x4*)a = v; }
DEV u32x2 ldstr(u32 a) { return __builtin_bit_cast(u32x2, __builtin_amdgcn_ds_read_tr16_b64_v4bf16((LAS bf16x4*)a)); }
DEV u32x4 ldstr2(u32 a0, u32 a1) { u32x2 x = ldstr(a0), y = ldstr(a1); return u32x4{x[0], x[1], y[0], y[1]}; }
DEV float ex2(float x) { return __builtin_amdgcn_exp2f(x); }

DEV float xor32_sum(float v) { auto r = __builtin_amdgcn_permlane32_swap(__float_as_uint(v), __float_as_uint(v), false, false); return __uint_as_float(r[0]) + __uint_as_float(r[1]); }
DEV float xor32_max(float v) { auto r = __builtin_amdgcn_permlane32_swap(__float_as_uint(v), __float_as_uint(v), false, false); return fmaxf(__uint_as_float(r[0]), __uint_as_float(r[1])); }
DEV float xor16_sum(float v) { auto r = __builtin_amdgcn_permlane16_swap(__float_as_uint(v), __float_as_uint(v), false, false); return __uint_as_float(r[0]) + __uint_as_float(r[1]); }
DEV float xor16_max(float v) { auto r = __builtin_amdgcn_permlane16_swap(__float_as_uint(v), __float_as_uint(v), false, false); return fmaxf(__uint_as_float(r[0]), __uint_as_float(r[1])); }
template <int CTRL> DEV float dpp(float v) { return __uint_as_float(__builtin_amdgcn_update_dpp(0u, __float_as_uint(v), CTRL, 0xF, 0xF, true)); }
constexpr int ROR8 = 0x128, ROR4 = 0x124, ROR2 = 0x122, ROR1 = 0x121;

#define STAMP(k)

#define POLL(N, V, CHECK, SLEEP, LOADS)                                  \
  _Pragma("unroll") for (int i = 0; i < N; ++i) pin(V[i]);               \
  _Pragma("nounroll") for (u32 sp = 0; sp < SPIN_CAP; ++sp) {            \
    u32 bad = 0;                                                         \
    _Pragma("unroll") for (int i = 0; i < N; ++i) bad |= CHECK;          \
    if (__builtin_amdgcn_ballot_w64(bad != 0) == 0) break;               \
    if (SLEEP) __builtin_amdgcn_s_sleep(SLEEP);                          \
    _Pragma("unroll") for (int i = 0; i < N; ++i) LOADS;                 \
    waitv<0>();                                                          \
    _Pragma("unroll") for (int i = 0; i < N; ++i) pin(V[i]);             \
  }
DEV void bar() { waitl(); __builtin_amdgcn_s_barrier(); CFENCE(); }
DEV bool allok(u32 bad) { return __builtin_amdgcn_ballot_w64(bad != 0) == 0; }
DEV u32 swz(int r) { return ((r & 3) << 2) | ((r >> 2) & 3); }

DEV void qpart(u32 wl, u32x4 xf, int lane) {
  const int g = lane >> 4, n = lane & 15, qq = n >> 2, p = lane & 3;
  f32x4 acc[4];
#pragma unroll
  for (int t = 0; t < 4; ++t) {
    const u32 a = wl + (8 * g + qq) * 128 + t * 32 + p * 8;
    acc[t] = mfma16(ldstr2(a, a + 512), xf, f32x4{0.f, 0.f, 0.f, 0.f});
  }
  CFENCE();
#pragma unroll
  for (int t = 0; t < 2; ++t) {
    const f32x4 u = acc[t], v = acc[2 + t];
    const bool lo = n < 8;
    *(LAS f32x4*)(wl + (n & 7) * 256 + ((n >> 3) * 8 + t * 4 + g) * 16) = f32x4{lo ? u[0] : v[0], lo ? u[1] : v[1], lo ? u[2] : v[2], lo ? u[3] : v[3]};
  }
}
DEV u32x4 qsum(u32 qr, u32 tl, u32 th) {
  f32x4 q[8];
#pragma unroll
  for (int i = 0; i < 8; ++i) q[i] = *(LAS f32x4*)(qr + i * 4096);
  const f32x4 r = ((q[0] + q[1]) + (q[2] + q[3])) + ((q[4] + q[5]) + (q[6] + q[7]));
  return u32x4{pk(r[0], r[1]), pk(r[2], r[3]), tl, th};
}

__global__ __launch_bounds__(512, 2) void gm_kernel(Params P) {
  extern __shared__ __attribute__((aligned(16))) char smem[];
  const int tid = threadIdx.x, lane = tid & 63;
  const int w = __builtin_amdgcn_readfirstlane(tid >> 6);
  const int wg = blockIdx.x, b = wg & 7, idx = (wg >> 3) & 15;
  const int h = HMAP, jq = idx >> 2;
  const rsrc_t ws = __builtin_amdgcn_make_buffer_rsrc(P.ws, 0, WS_BYTES, 0x00020000);
  const int n = lane & 15, g = lane >> 4, qq = n >> 2, p = lane & 3;
  STAMP(0);
  (void)smem;
  if (wg < 128) {
    const int hkv = idx >> 1, half = idx & 1;
    u32x4 ep, xf;
    ldg_sc1(ep, 0u, P.ws);
    const size_t qoff = (size_t)((jq >> 1) * 256 + w * 32) * 8192 + h * 256 + (jq & 1) * 128;
#pragma unroll
    for (int s = 0; s < 4; ++s) dma_nt((u32)((lane >> 3) * 8192 + (lane & 7) * 16), P.wqa + qoff + s * 65536, L_WQA + w * 4096 + s * 1024);
    ldg(xf, (u32)((lane & 7) * 1024 + g * 16), P.x + (jq >> 1) * 512 + w * 64);
    bar();
    if (w < 2) {
#pragma unroll
      for (int k = 0; k < 16; ++k)
        dma_nt((u32)(g * 8192 + ((n ^ k) * 16)), P.wuk + (size_t)(jq * 128 + w * 64 + k * 4) * 8192 + h * 256, L_WUK + w * 16384 + k * 1024);
      waitv<16>();
    } else {
      waitv<0>();
    }
    pin(ep); pin(xf);
    const u64 epoch = (((u64)__builtin_amdgcn_readfirstlane(ep[1]) << 32) | (u32)__builtin_amdgcn_readfirstlane(ep[0])) + 0x0101010101010101ull;
    const u32 tl = (u32)epoch, th = (u32)(epoch >> 32);
    STAMP(1);
    qpart(L_WQA + w * 4096, xf, lane);
    bar();
    if ((w >> 1) == 1) wsst(ws, WS_QA + ((h * 2 + (jq & 1)) * 2 + (jq >> 1)) * 2048 + (w & 1) * 1024 + lane * 16, qsum(L_WQA + (w & 1) * 1024 + lane * 16, tl, th));
    STAMP(2);
    if (w < 2) {
      u32x4 uk[16], v[9];
      waitv<0>();
#pragma unroll
      for (int t = 0; t < 4; ++t)
#pragma unroll
        for (int s = 0; s < 4; ++s) uk[t * 4 + s] = lds128(L_WUK + w * 16384 + (4 * n + t) * 256 + (((4 * s + g) ^ n) * 16));
      v[0][2] = ~tl;
      POLL(9, v, (v[i][2] ^ tl) | (v[i][3] ^ th), 0,
           (i < 8 ? ldg_sc1(v[i], (u32)(n * 256 + g * 32), P.ws + WS_QA + h * 8192 + (i >> 2) * 4096 + ((i >> 1) & 1) * 128 + (i & 1) * 16)
                  : ldg_sc1(v[i], (u32)((lane & 7) * 16), P.ws + WS_FQ + (h >> 1) * 128)))
      STAMP(3);
      f32x4 d[4];
#pragma unroll
      for (int t = 0; t < 4; ++t) {
        d[t] = f32x4{0.f, 0.f, 0.f, 0.f};
#pragma unroll
        for (int s = 0; s < 4; ++s) d[t] = mfma16(u32x4{v[2 * s][0], v[2 * s][1], v[2 * s + 1][0], v[2 * s + 1][1]}, uk[t * 4 + s], d[t]);
      }
#pragma unroll
      for (int r = 0; r < 2; ++r) {
        float e[4];
#pragma unroll
        for (int t = 0; t < 4; ++t) {
          auto s2 = __builtin_amdgcn_permlane32_swap(__float_as_uint(d[t][r]), __float_as_uint(d[t][2 + r]), false, false);
          e[t] = (__uint_as_float(s2[0]) + __uint_as_float(s2[1])) * SCL;
        }
        const int bb = 4 * (g & 1) + 2 * (g >> 1) + r;
        wsst2(ws, WS_QL + (bb * 32 + h) * 1024 + jq * 256 + w * 128 + n * 8, u32x2{clampw(pk(e[0], e[1])), clampw(pk(e[2], e[3]))});
      }
    }
    bar();
    STAMP(4);
#pragma unroll
    for (int s = 0; s < 4; ++s) dma_nt((u32)((lane >> 3) * 8192 + (lane & 7) * 16), P.wqb + qoff + s * 65536, L_WQB + w * 4096 + s * 1024);
#pragma unroll
    for (int k = 0; k < 4; ++k) wsst(ws, WS_AP + (h * 4 + jq) * 32768 + (w * 4 + k) * 1024 + lane * 16, u32x4{SENT, SENT, SENT, SENT});
    const size_t kvoff = ((size_t)(b * 8 + hkv) * 4096 + 3072 + half * 512 + w * 64) * 256;
    u32x4 vv[16], pf[2];
    float mx, l;
    f32x4 o[8];
#define KV_ISSUE(I0, I1)                                                                                                              \
  _Pragma("unroll") for (int i = I0; i < I1; ++i) {                                                                                   \
    if (i <= NTHIN) waitv<0>(); else if (i >= NTHIN + WB) waitv<WB - 1>();                                                            \
    if (i < 16) dma_nt((u32)(g * 256 + ((n ^ ((4 * i + g) & 15)) * 16)), P.kc + kvoff + i * 1024, (u32)(w * 16384 + i * 1024));       \
    else ldg_nt(vv[i & 15], (u32)(g * 1024 + n * 16), P.vc + kvoff + (32 * ((i >> 3) & 1) + 16 * ((i >> 2) & 1) + (i & 3)) * 256);    \
  }
#define PV_STEP(gm)                                                                                   \
  _Pragma("unroll") for (int k = 0; k < 8; ++k) pin(vv[gm * 8 + k]);                                  \
  _Pragma("unroll") for (int c = 0; c < 4; ++c) {                                                     \
    u32x4 t0, t1;                                                                                     \
    _Pragma("unroll") for (int jp = 0; jp < 4; ++jp) {                                                \
      const u32 a = vv[gm * 8 + 2 * jp][c], bq = vv[gm * 8 + 2 * jp + 1][c];                          \
      t0[jp] = __builtin_amdgcn_perm(bq, a, 0x05040100);                                              \
      t1[jp] = __builtin_amdgcn_perm(bq, a, 0x07060302);                                              \
    }                                                                                                 \
    o[2 * c] = mfma16(pf[gm], t0, gm ? o[2 * c] : f32x4{0.f, 0.f, 0.f, 0.f});                         \
    o[2 * c + 1] = mfma16(pf[gm], t1, gm ? o[2 * c + 1] : f32x4{0.f, 0.f, 0.f, 0.f});                 \
  }
    KV_ISSUE(0, 1)
    qpart(L_WQB + w * 4096, xf, lane);
    bar();
    if ((w >> 1) == 1) wsst(ws, WS_QB + ((((w & 1) * 4 + g) * 2 + (jq >> 1)) * 32 + h) * 512 + (jq & 1) * 256 + n * 16, qsum(L_WQB + (w & 1) * 1024 + lane * 16, tl, th));
    if (w == 4 && lane == 0) wsst(ws, WS_FP + (jq * 32 + h) * 16, u32x4{0, 0, tl, th});
    STAMP(5);
    KV_ISSUE(1, 12)
    if (w == 0) {
      u32x4 v[4];
      v[0][2] = ~tl;
      POLL(4, v, (v[i][2] ^ tl) | (v[i][3] ^ th), 0, ldg_sc1(v[i], (u32)(lane * 16), P.ws + WS_QB + ((b * 2 + (i >> 1)) * 32 + hkv * 4) * 512 + (i & 1) * 1024))
#pragma unroll
      for (int i = 0; i < 2; ++i)
        *(LAS u32x2*)(L_QBS + i * 512 + lane * 8) = u32x2{pk(lo2f(v[i][0]) + lo2f(v[2 + i][0]), hi2f(v[i][0]) + hi2f(v[2 + i][0])), pk(lo2f(v[i][1]) + lo2f(v[2 + i][1]), hi2f(v[i][1]) + hi2f(v[2 + i][1]))};
    }
    KV_ISSUE(12, 16 + WB)
      {
        STAMP(6);
        bar();
        u32x4 qb[4];
#pragma unroll
        for (int s = 0; s < 4; ++s) qb[s] = lds128(L_QBS + (n & 3) * 256 + s * 64 + g * 16);
        f32x4 sc[4];
        mx = -3.0e38f;
#pragma unroll
        for (int t = 0; t < 4; ++t) {
          sc[t] = f32x4{0.f, 0.f, 0.f, 0.f};
#pragma unroll
          for (int s = 0; s < 4; ++s) sc[t] = mfma16(lds128(w * 16384 + (t * 16 + n) * 256 + (((4 * s + g) ^ n) * 16)), qb[s], sc[t]);
          sc[t] *= SCL;
          mx = fmaxf(fmaxf(mx, fmaxf(sc[t][0], sc[t][1])), fmaxf(sc[t][2], sc[t][3]));
        }
        mx = xor32_max(xor16_max(mx));
        l = 0.f;
#pragma unroll
        for (int t = 0; t < 4; ++t)
#pragma unroll
          for (int e = 0; e < 4; ++e) { sc[t][e] = ex2(sc[t][e] - mx); l += sc[t][e]; }
        l = xor32_sum(xor16_sum(l));
#pragma unroll
        for (int gm = 0; gm < 2; ++gm)
          pf[gm] = u32x4{pk(sc[2 * gm][0], sc[2 * gm][1]), pk(sc[2 * gm][2], sc[2 * gm][3]), pk(sc[2 * gm + 1][0], sc[2 * gm + 1][1]), pk(sc[2 * gm + 1][2], sc[2 * gm + 1][3])};
        if (lane < 4) *(LAS f32x2*)(L_BML + lane * 64 + w * 8) = f32x2{mx, l};
        STAMP(7);
      }
    KV_ISSUE(16 + WB, 32)
    waitv<8>();
    PV_STEP(0)
    waitv<0>();
    STAMP(8);
    PV_STEP(1)
    if (lane < 16) {
#pragma unroll
      for (int hh = 0; hh < 4; ++hh) {
        *(LAS f32x4*)(L_BM + w * 2048 + hh * 512 + n * 32) = f32x4{o[0][hh], o[1][hh], o[2][hh], o[3][hh]};
        *(LAS f32x4*)(L_BM + w * 2048 + hh * 512 + n * 32 + 16) = f32x4{o[4][hh], o[5][hh], o[6][hh], o[7][hh]};
      }
    }
    bar();
    if (w < 2) {
      const int hh = 2 * w + (lane >> 5), un = lane & 31;
      f32x4 ml[4];
#pragma unroll
      for (int i = 0; i < 4; ++i) ml[i] = *(LAS f32x4*)(L_BML + hh * 64 + i * 16);
      float M = ml[0][0];
#pragma unroll
      for (int i = 1; i < 8; ++i) M = fmaxf(M, ml[i >> 1][(i & 1) * 2]);
      float L = 0.f;
      f32x4 r = {0.f, 0.f, 0.f, 0.f};
#pragma unroll
      for (int i = 0; i < 8; ++i) {
        const float e = ex2(ml[i >> 1][(i & 1) * 2] - M);
        L += e * ml[i >> 1][(i & 1) * 2 + 1];
        r += e * *(LAS f32x4*)(L_BM + i * 2048 + hh * 512 + un * 16);
      }
      wsst(ws, WS_BP + (((hkv * 4 + hh) * 2 + half) * 8 + b) * 512 + un * 16, u32x4{pk(r[0], r[1]), pk(r[2], r[3]), tl, __float_as_uint((un & 1) ? L : M)});
    }
    STAMP(9);
  } else {
    const char* cb = P.ckv + ((size_t)b * 4096 + idx * 256) * 1024;
    u32x4 ep, gx[2], sg[16], v[4], fl[1];
    ldg_sc1(ep, 0u, P.ws);
    if (w == 7) {
#pragma unroll
      for (int k = 0; k < 2; ++k) wsst(ws, WS_QL + (b * 32 + 2 * idx + k) * 1024 + lane * 16, u32x4{SENT, SENT, SENT, SENT});
    }
    ldg(gx[0], (u32)(lane * 16), P.gw);
    ldg(gx[1], (u32)(lane * 16), P.x + w * 1024);
#define CKV_ISSUE(I0, I1)                                                                                   \
  _Pragma("unroll") for (int i = I0; i < I1; ++i) {                                                         \
    if (i >= WA) waitv<WA - 1>();                                                                           \
    dma_nt((u32)((lane ^ swz(i * 8 + w)) * 16), cb + (i * 8 + w) * 1024, (u32)((i * 8 + w) * 1024));        \
  }
    CKV_ISSUE(0, WA + 1)
    pin(ep); pin(gx[0]); pin(gx[1]);
    const u64 epoch = (((u64)__builtin_amdgcn_readfirstlane(ep[1]) << 32) | (u32)__builtin_amdgcn_readfirstlane(ep[0])) + 0x0101010101010101ull;
    const u32 tl = (u32)epoch, th = (u32)(epoch >> 32);
    if (w == 7 && lane == 0) wsst(ws, WS_FQ + (idx * 8 + b) * 16, u32x4{0, 0, tl, th});
    CKV_ISSUE(WA + 1, 16)
#define FQ_LOAD ldg_sc1(fl[0], (u32)((lane & 1) * 128), P.ws + WS_FQ + (2 * w * 8 + b) * 16)
    FQ_LOAD;
    bar();
    STAMP(1);
    float gate;
    {
      float a = 0.f;
#pragma unroll
      for (int e = 0; e < 4; ++e) a += rbf(lo2f(gx[1][e]) * lo2f(gx[0][e])) + rbf(hi2f(gx[1][e]) * hi2f(gx[0][e]));
      a += dpp<ROR1>(a); a += dpp<ROR2>(a); a += dpp<ROR4>(a); a += dpp<ROR8>(a);
      a = xor32_sum(xor16_sum(a));
      gate = rbf(1.f / (1.f + __expf(-rbf(a))));
    }
#define QL_LOAD ldg_sc1(v[i], (u32)(lane * 16), P.ws + WS_QL + (b * 32 + 4 * w + i) * 1024)
#define SG_LOAD ldg_nt(sg[i], (u32)((lane ^ swz(i * 8 + w)) * 16), cb + (128 + i * 8 + w) * 1024)
#pragma unroll
    for (int i = 0; i < NPRE; ++i) { waitv<WA - 1>(); SG_LOAD; }
    waitv<NPRE>();
    POLL(1, fl, (fl[i][2] ^ tl) | (fl[i][3] ^ th), 1, FQ_LOAD)
    bool got = false;
#pragma unroll
    for (int a = NPRE; a < 16; a += NSTEP) {
      if (!got) {
#pragma unroll
        for (int i = 0; i < 4; ++i) QL_LOAD;
      }
#pragma unroll
      for (int i = a; i < a + NSTEP; ++i) SG_LOAD;
      if (!got) {
        waitv<NSTEP>();
        u32 m = 0;
#pragma unroll
        for (int i = 0; i < 4; ++i) { pin(v[i]); m = vmax4(m, v[i]); }
        got = allok(m == SENT);
      }
    }
    POLL(4, v, (u32)(vmax4(0, v[i]) == SENT), 1, QL_LOAD)
    STAMP(2);
#pragma unroll
    for (int i = 0; i < 4; ++i) lds128w(L_X + (4 * w + i) * 1024 + ((lane ^ swz(4 * w + i)) * 16), v[i]);
    bar();
    const int r32 = lane & 31, hf = lane >> 5, kg = w & 3, hh2 = w >> 2;
    const u32 fb = n * 1024 + 16 * ((4 * g) ^ swz(n));
    u32x4 qf[16];
#pragma unroll
    for (int ks = 0; ks < 16; ++ks) qf[ks] = lds128(L_X + hh2 * 16384 + (fb ^ (16 * (ks & 3))) + 256 * (ks >> 2));
    f32x16 o[2];
    float m_run = -3.0e38f, m_sc = -3.0e38f, l_run = 0.f;
#pragma unroll
    for (int ph = 0; ph < 2; ++ph) {
      f32x4 sc[2] = {{0.f, 0.f, 0.f, 0.f}, {0.f, 0.f, 0.f, 0.f}};
#pragma unroll
      for (int ks = 0; ks < 16; ++ks)
#pragma unroll
        for (int t = 0; t < 2; ++t) sc[t] = mfma16(lds128(kg * 32768 + t * 16384 + (fb ^ (16 * (ks & 3))) + 256 * (ks >> 2)), qf[ks], sc[t]);
      float mx = fmaxf(fmaxf(fmaxf(sc[0][0], sc[0][1]), fmaxf(sc[0][2], sc[0][3])), fmaxf(fmaxf(sc[1][0], sc[1][1]), fmaxf(sc[1][2], sc[1][3])));
      mx = xor32_max(xor16_max(mx));
      if (ph == 0) bar();
      if (lane < 16) *(LAS float*)(L_MAXB + (hh2 * 16 + n) * 16 + kg * 4) = mx;
      bar();
      const f32x4 ma = *(LAS f32x4*)(L_MAXB + (hh2 * 16 + n) * 16), mb = *(LAS f32x4*)(L_MAXB + r32 * 16);
      mx = fmaxf(fmaxf(fmaxf(ma[0], ma[1]), fmaxf(ma[2], ma[3])), m_sc);
      float l = 0.f;
#pragma unroll
      for (int t = 0; t < 2; ++t) {
#pragma unroll
        for (int i = 0; i < 4; ++i) { sc[t][i] = ex2(sc[t][i] - mx); l += sc[t][i]; }
        *(LAS u32x2*)(L_P + (2 * kg + t) * 1024 + ((g & 1) * 32 + hh2 * 16 + n) * 16 + (g >> 1) * 8) = u32x2{pk(sc[t][0], sc[t][1]), pk(sc[t][2], sc[t][3])};
      }
      l_run = l_run * ex2(m_sc - mx) + l;
      m_sc = mx;
      if (ph == 1) {
        const float ls = xor32_sum(xor16_sum(l_run));
        if (lane < 16) *(LAS float*)(L_SUMB + (hh2 * 16 + n) * 16 + kg * 4) = ls;
      }
      const float mo = fmaxf(fmaxf(fmaxf(mb[0], mb[1]), fmaxf(mb[2], mb[3])), m_run);
      const float alpha = ex2(m_run - mo);
      m_run = mo;
#pragma unroll
      for (int ct = 0; ct < 2; ++ct)
#pragma unroll
        for (int i = 0; i < 16; ++i) o[ct][i] = ph ? o[ct][i] * alpha : 0.f;
      bar();
      STAMP(3 + 3 * ph);
      if (ph == 1) {
#pragma unroll
        for (int i = 4; i < 8; ++i) ldg_nt(sg[i], (u32)(g * 1024 + n * 16), P.woa + (size_t)(h * 128 + (4 * w + (i & 3)) * 4) * 1024 + jq * 256);
      }
      if (ph == 1 && w == 0) {
        const f32x4 a = *(LAS f32x4*)(L_SUMB + r32 * 16);
        const float lsum = (a[0] + a[1]) + (a[2] + a[3]);
        if (lane < 32) wsst(ws, WS_ML + ((b * 16 + idx) * 32 + lane) * 16, u32x4{__float_as_uint(m_run), __float_as_uint(lsum), tl, th});
      }
      {
        const int blk = g & 1;
        const u32 a0 = ((4 * hf + qq) * 1024 + (w >> 1) * 256 + 16 * ((2 * blk + (p >> 1)) ^ ((qq << 2) | hf)) + 8 * (p & 1)) ^ (128 * (w & 1));
        const u32 a1 = (a0 ^ 32) + 8192;
#pragma unroll
        for (int ks = 0; ks < 8; ++ks) {
          const u32x4 pf = lds128(L_P + ks * 1024 + lane * 16);
#pragma unroll
          for (int ct = 0; ct < 2; ++ct) o[ct] = mfma32(ldstr2((a0 ^ (64 * ct)) + 16384 * ks, (a1 ^ (64 * ct)) + 16384 * ks), pf, o[ct]);
        }
      }
      if (ph == 0) {
        bar();
        STAMP(4);
        waitv<0>();
#pragma unroll
        for (int i = 0; i < 16; ++i) { pin(sg[i]); lds128w((i * 8 + w) * 1024 + lane * 16, sg[i]); }
#define FP_LOAD ldg_sc1(fl[0], (u32)(r32 * 16), P.ws + WS_FP + (w >> 1) * 512)
        waitl();
        FP_LOAD;
#pragma unroll
        for (int i = 0; i < 4; ++i) ldg_nt(sg[i], (u32)(g * 8192 + n * 16), P.wuv + (size_t)(jq * 128 + (4 * w + i) * 4) * 8192 + h * 256);
        bar();
        STAMP(5);
      }
    }
    bar();
    STAMP(7);
#pragma unroll
    for (int ct = 0; ct < 2; ++ct)
#pragma unroll
      for (int uu = 0; uu < 2; ++uu)
        lds128w(L_X + w * 4096 + r32 * 128 + (((ct * 4 + hf * 2 + uu) ^ (r32 & 7)) * 16),
                clamp4(u32x4{pk(o[ct][8 * uu], o[ct][8 * uu + 1]), pk(o[ct][8 * uu + 2], o[ct][8 * uu + 3]), pk(o[ct][8 * uu + 4], o[ct][8 * uu + 5]), pk(o[ct][8 * uu + 6], o[ct][8 * uu + 7])}));
    if (lane == 0) *(LAS float*)(L_CL + 4096 + w * 4) = gate;
    waitv<8>();
    POLL(1, fl, (fl[i][2] ^ tl) | (fl[i][3] ^ th), 1, FP_LOAD)
    waitl();
#pragma unroll
    for (int k = 0; k < 4; ++k) {
      const int hh = 8 * k + (lane >> 3);
      wsst(ws, WS_AP + (((hh * 4 + (w >> 1)) * 8 + b) * 16 + idx) * 256 + (w & 1) * 128 + (lane & 7) * 16, lds128(L_X + w * 4096 + hh * 128 + (((lane & 7) ^ (hh & 7)) * 16)));
    }
    STAMP(8);
    u32x4 wu[4], wa[4];
    {
      u32x4 mlv[1];
      ldg_sc1(mlv[0], (u32)(n * 512), P.ws + WS_ML + (w * 512 + h) * 16);
      waitv<5>();
#pragma unroll
      for (int i = 0; i < 8; ++i) { pin(sg[i]); lds128w((i >> 2) * 32768 + (4 * w + (i & 3)) * 1024 + lane * 16, sg[i]); }
      bar();
#pragma unroll
      for (int i = 0; i < 4; ++i) {
        const u32 a = L_WUV + (32 * i + 16 * (g & 1) + 4 * (g >> 1) + qq) * 256 + w * 32 + p * 8;
        wu[i] = ldstr2(a, a + 2048);
        const u32 a2 = L_WOA + (32 * i + 8 * g + qq) * 256 + w * 32 + p * 8;
        wa[i] = ldstr2(a2, a2 + 1024);
      }
      waitv<0>();
      POLL(1, mlv, (mlv[i][2] ^ tl) | (mlv[i][3] ^ th), 2, ldg_sc1(mlv[i], (u32)(n * 512), P.ws + WS_ML + (w * 512 + h) * 16))
      const u32x4 ml = mlv[0];
#pragma unroll
      for (int i = 0; i < 4; ++i) ldg_sc1(v[i], (u32)(lane * 16), P.ws + WS_AP + ((h * 4 + jq) * 8 + w) * 4096 + i * 1024);
      const float ms = __uint_as_float(ml[0]);
      float M = ms;
      M = fmaxf(M, dpp<ROR1>(M)); M = fmaxf(M, dpp<ROR2>(M)); M = fmaxf(M, dpp<ROR4>(M)); M = fmaxf(M, dpp<ROR8>(M));
      const float e = ex2(ms - M);
      float L = e * __uint_as_float(ml[1]);
      L += dpp<ROR1>(L); L += dpp<ROR2>(L); L += dpp<ROR4>(L); L += dpp<ROR8>(L);
      const float wgt = e / L;
      float wv[4], acc[8];
#pragma unroll
      for (int i = 0; i < 4; ++i) wv[i] = __uint_as_float(__builtin_amdgcn_ds_bpermute((4 * i + g) * 4, __float_as_uint(wgt)));
      waitv<0>();
      POLL(4, v, (u32)(vmax4(0, v[i]) == SENT), 2, ldg_sc1(v[i], (u32)(lane * 16), P.ws + WS_AP + ((h * 4 + jq) * 8 + w) * 4096 + i * 1024))
      STAMP(9);
#pragma unroll
      for (int i = 0; i < 4; ++i) dma_nt((u32)(g * 1024 + n * 16), P.wob + (size_t)(h * 128 + (4 * w + i) * 4) * 1024 + jq * 256, L_WOB + (4 * w + i) * 1024);
#pragma unroll
      for (int i = 0; i < 4; ++i) {
#pragma unroll
        for (int e2 = 0; e2 < 4; ++e2) {
          acc[2 * e2] = (i ? acc[2 * e2] : 0.f) + wv[i] * lo2f(v[i][e2]);
          acc[2 * e2 + 1] = (i ? acc[2 * e2 + 1] : 0.f) + wv[i] * hi2f(v[i][e2]);
        }
      }
      float r4[4], r2[2];
#pragma unroll
      for (int e2 = 0; e2 < 4; ++e2) {
        auto r = __builtin_amdgcn_permlane32_swap(__float_as_uint(acc[e2]), __float_as_uint(acc[4 + e2]), false, false);
        r4[e2] = __uint_as_float(r[0]) + __uint_as_float(r[1]);
      }
#pragma unroll
      for (int e2 = 0; e2 < 2; ++e2) {
        auto r = __builtin_amdgcn_permlane16_swap(__float_as_uint(r4[e2]), __float_as_uint(r4[2 + e2]), false, false);
        r2[e2] = __uint_as_float(r[0]) + __uint_as_float(r[1]);
      }
      *(LAS u32*)(L_CL + (n * 8 + w) * 16 + g * 4) = pk(r2[0], r2[1]);
    }
    bar();
    STAMP(10);
    {
      f32x4 d = {0.f, 0.f, 0.f, 0.f};
#pragma unroll
      for (int ks = 0; ks < 4; ++ks) {
        d = mfma16(wu[ks], lds128(L_CL + ((ks * 4 + g) * 8 + (n & 7)) * 16), d);
      }
      if (n < 8) wsst(ws, WS_CT + (h * 4 + jq) * 4096 + (w * 4 + g) * 128 + n * 16, u32x4{pk(d[0], d[1]), pk(d[2], d[3]), tl, th});
    }
    STAMP(11);
    {
      u32x4 wb[4];
      const f32x4 gg = *(LAS f32x4*)(L_CL + 4096 + (g & 1) * 16);
      waitv<0>();
      v[0][2] = ~tl;
      POLL(2, v, (v[i][2] ^ tl) | (v[i][3] ^ th), 0, ldg_sc1(v[i], (u32)(hf * 4096 + r32 * 16), P.ws + WS_CT + (h * 4 + 2 * i) * 4096 + w * 512))
      u32x4 bp[1];
      ldg_sc1(bp[0], (u32)(hf * 4096 + r32 * 16), P.ws + WS_BP + h * 8192 + w * 512);
      {
        auto s0 = __builtin_amdgcn_permlane32_swap(__float_as_uint(lo2f(v[0][0]) + lo2f(v[1][0])), __float_as_uint(lo2f(v[0][1]) + lo2f(v[1][1])), false, false);
        auto s1 = __builtin_amdgcn_permlane32_swap(__float_as_uint(hi2f(v[0][0]) + hi2f(v[1][0])), __float_as_uint(hi2f(v[0][1]) + hi2f(v[1][1])), false, false);
        *(LAS u32*)(L_CL + 8192 + ((2 * w + (r32 >> 4)) * 8 + (r32 & 7)) * 16 + ((r32 >> 3) & 1) * 8 + hf * 4) =
            pk(__uint_as_float(s0[0]) + __uint_as_float(s0[1]), __uint_as_float(s1[0]) + __uint_as_float(s1[1]));
      }
      bar();
      STAMP(12);
      f32x4 da = {0.f, 0.f, 0.f, 0.f};
#pragma unroll
      for (int i = 0; i < 4; ++i) da = mfma16(lds128(L_CL + 8192 + ((4 * i + g) * 8 + (n & 7)) * 16), wa[i], da);
      da *= gg;
#pragma unroll
      for (int i = 0; i < 4; ++i) {
        const u32 a = (32 * i + 8 * g + qq) * 256 + w * 32 + p * 8;
        wb[i] = ldstr2(L_WOB + a, L_WOB + a + 1024);
      }
      waitv<0>();
      POLL(1, bp, bp[i][2] ^ tl, 1, ldg_sc1(bp[i], (u32)(hf * 4096 + r32 * 16), P.ws + WS_BP + h * 8192 + w * 512))
      {
        const float x = __uint_as_float(bp[0][3]), y = dpp<0x141>(x);
        const float m0 = (lane & 1) ? y : x, l0 = (lane & 1) ? x : y;
        const float e = ex2(m0 - xor32_max(m0));
        const float wgt = e * rbf(1.f - gate) / xor32_sum(e * l0);
        auto s0 = __builtin_amdgcn_permlane32_swap(__float_as_uint(wgt * lo2f(bp[0][0])), __float_as_uint(wgt * lo2f(bp[0][1])), false, false);
        auto s1 = __builtin_amdgcn_permlane32_swap(__float_as_uint(wgt * hi2f(bp[0][0])), __float_as_uint(wgt * hi2f(bp[0][1])), false, false);
        *(LAS u32*)(L_CL + 12288 + ((r32 >> 1) * 8 + w) * 16 + (r32 & 1) * 8 + hf * 4) =
            pk(__uint_as_float(s0[0]) + __uint_as_float(s0[1]), __uint_as_float(s1[0]) + __uint_as_float(s1[1]));
      }
      bar();
      STAMP(13);
#pragma unroll
      for (int i = 0; i < 4; ++i) da = mfma16(lds128(L_CL + 12288 + ((4 * i + g) * 8 + (n & 7)) * 16), wb[i], da);
      u32x4 ov;
#pragma unroll
      for (int r = 0; r < 4; ++r) ov[r] = (__float_as_uint(da[r]) & 0xFFFFFF00u) | ((tl >> (8 * r)) & 0xFFu);
      if (lane < 32) wsst(ws, WS_OP + ((jq * 32 + w * 4 + (lane >> 3)) * 32 + h) * 128 + (lane & 7) * 16, ov);
      STAMP(14);
      if (w == 0) {
        const int red = wg - 128;
        waitv<0>();
        v[0][0] = ~tl;
        POLL(4, v, __builtin_amdgcn_perm(__builtin_amdgcn_perm(v[i][3], v[i][2], 0x0c0c0400), __builtin_amdgcn_perm(v[i][1], v[i][0], 0x0c0c0400), 0x05040100) ^ tl, 0,
             ldg_sc1(v[i], (u32)(lane * 16), P.ws + WS_OP + red * 4096 + i * 1024))
        float r[4];
#pragma unroll
        for (int e2 = 0; e2 < 4; ++e2) {
          float x = (__uint_as_float(v[0][e2] & 0xFFFFFF00u) + __uint_as_float(v[1][e2] & 0xFFFFFF00u)) + (__uint_as_float(v[2][e2] & 0xFFFFFF00u) + __uint_as_float(v[3][e2] & 0xFFFFFF00u));
          x += dpp<ROR8>(x);
          r[e2] = xor32_sum(xor16_sum(x));
        }
        const float rv = g == 0 ? r[0] : g == 1 ? r[1] : g == 2 ? r[2] : r[3];
        const int L = red & 7, col = (red >> 3) * 32 + 16 * (L >> 2) + 8 * (L & 1) + n, bb = 4 * ((L >> 1) & 1) + g;
        if (n < 8) *(unsigned short*)(P.out + (bb * 512 + col) * 2) = (unsigned short)pk(rv, 0.f);
        if (wg == 128 && lane == 0) wsst2(ws, WS_EPOCH, u32x2{tl, th});
        STAMP(15);
      }
    }
  }
}

extern "C" void kernel_launch(void* const* d_in, const int* in_sizes, int n_in, void* d_out, int out_size, void* d_ws, size_t ws_size, hipStream_t stream) {
  Params p;
  p.x = (const char*)d_in[0]; p.ckv = (const char*)d_in[1]; p.wqa = (const char*)d_in[2]; p.wuk = (const char*)d_in[3];
  p.wuv = (const char*)d_in[4]; p.woa = (const char*)d_in[5]; p.kc = (const char*)d_in[7]; p.vc = (const char*)d_in[8];
  p.wqb = (const char*)d_in[9]; p.wob = (const char*)d_in[10]; p.gw = (const char*)d_in[11];
  p.out = (char*)d_out; p.ws = (char*)d_ws;
  (void)hipFuncSetAttribute((const void*)gm_kernel, hipFuncAttributeMaxDynamicSharedMemorySize, LDS_BYTES);
  hipLaunchKernelGGL(gm_kernel, dim3(256), dim3(512), LDS_BYTES, stream, p);
}
```

```cpp
#pragma clang diagnostic ignored "-Wint-to-pointer-cast"
#include <hip/hip_runtime.h>
#include <hip/hip_bf16.h>
#include <stdint.h>

typedef unsigned int u32;
typedef unsigned long long u64;
typedef u32 u32x4 __attribute__((ext_vector_type(4)));
typedef u32 u32x2 __attribute__((ext_vector_type(2)));
typedef float f32x4 __attribute__((ext_vector_type(4)));
typedef float f32x2 __attribute__((ext_vector_type(2)));
typedef float f32x16 __attribute__((ext_vector_type(16)));
typedef __bf16 bf16x8 __attribute__((ext_vector_type(8)));
typedef __bf16 bf16x4 __attribute__((ext_vector_type(4)));
typedef __bf16 bf16x2 __attribute__((ext_vector_type(2)));
#define LAS __attribute__((address_space(3)))
#define DEV __device__ __forceinline__

#define HMAP (b + 8 * (idx & 3))
#define WA 4
#define NPRE 8
#define NSTEP 4
#define NMID 0
#define WB 4
#define BDLY 32
#define WOBR 0
#define VX 0
#define NE 0
#define WE 1

constexpr u32 WS_EPOCH = 0;
constexpr u32 WS_FQ   = 4096;
constexpr u32 WS_FP   = 8192;
constexpr u32 WS_QA   = 16384;
constexpr u32 WS_QB   = WS_QA + 262144;
constexpr u32 WS_QL   = WS_QB + 262144;
constexpr u32 WS_ML   = WS_QL + 262144;
constexpr u32 WS_CT   = WS_ML + 65536;
constexpr u32 WS_BP   = WS_CT + 524288;
constexpr u32 WS_OP   = WS_BP + 262144;
constexpr u32 WS_AP   = WS_OP + 524288;
constexpr u32 WS_BYTES = WS_AP + 4194304;
constexpr u32 WS_DBG = 16u << 20;

constexpr u32 L_X = 131072;
constexpr u32 L_P = L_X, L_MAXB = L_X + 8192, L_SUMB = L_X + 8704;
constexpr u32 L_WUV = 0, L_WOA = 32768, L_WOB = 65536, L_CL = 98304;
constexpr u32 L_WUK = 0, L_WQA = 65536, L_WQB = L_X;
constexpr u32 L_BM = L_X + 16384, L_BML = L_X + 3072, L_QBS = L_X + 2048;
constexpr u32 LDS_BYTES = 163840;
constexpr u32 SENT = 0xFFFFFFFFu;
#define DMA_Q dma_nt
constexpr u32 SPIN_CAP = 1u << 20;
constexpr float SCL = 0.08838834764831845f * 1.4426950408889634f;

struct Params {
  const char *x, *ckv, *wqa, *wuk, *wuv, *woa, *kc, *vc, *wqb, *wob, *gw;
  char *out, *ws;
};

template <int N> DEV void waitv() { asm volatile("s_waitcnt vmcnt(%0)" ::"i"(N) : "memory"); }
DEV void waitl() { asm volatile("s_waitcnt lgkmcnt(0)" ::: "memory"); }
DEV void ldg_nt(u32x4& r, u32 voff, const char* sb) { asm volatile("global_load_dwordx4 %0, %1, %2 nt" : "=v"(r) : "v"(voff), "s"(sb) : "memory"); }
DEV void ldg(u32x4& r, u32 voff, const char* sb) { asm volatile("global_load_dwordx4 %0, %1, %2" : "=v"(r) : "v"(voff), "s"(sb) : "memory"); }
DEV void ldg_sc1(u32x4& r, u32 voff, const char* sb) { asm volatile("global_load_dwordx4 %0, %1, %2 sc1" : "=v"(r) : "v"(voff), "s"(sb) : "memory"); }
DEV void rel_sc1(u32x4& r, bool miss, u32 voff, const char* sb) {
  asm volatile("s_mov_b64 exec, %3\n\tglobal_load_dwordx4 %0, %1, %2 sc1\n\ts_mov_b64 exec, -1" : "+v"(r) : "v"(voff), "s"(sb), "s"(__builtin_amdgcn_ballot_w64(miss)) : "memory");
}
DEV void dma_nt(u32 voff, const char* sb, u32 lds) {
  u32 keep;
  asm volatile("s_mov_b32 %0, m0\n\ts_mov_b32 m0, %3\n\ts_nop 0\n\tglobal_load_lds_dwordx4 %1, %2 nt\n\ts_mov_b32 m0, %0" : "=&s"(keep) : "v"(voff), "s"(sb), "s"(lds) : "memory");
}
DEV void dma(u32 voff, const char* sb, u32 lds) {
  u32 keep;
  asm volatile("s_mov_b32 %0, m0\n\ts_mov_b32 m0, %3\n\ts_nop 0\n\tglobal_load_lds_dwordx4 %1, %2\n\ts_mov_b32 m0, %0" : "=&s"(keep) : "v"(voff), "s"(sb), "s"(lds) : "memory");
}
DEV void pin(u32x4& r) { asm volatile("" : "+v"(r)); }
#define CFENCE() asm volatile("" ::: "memory")

typedef __amdgpu_buffer_rsrc_t rsrc_t;
DEV u32x4 wsld(rsrc_t r, u32 off) { return __builtin_amdgcn_raw_buffer_load_b128(r, off, 0, 16); }
DEV u32x2 wsld2(rsrc_t r, u32 off) { return __builtin_amdgcn_raw_buffer_load_b64(r, off, 0, 16); }
DEV void wsst(rsrc_t r, u32 off, u32x4 v) { __builtin_amdgcn_raw_buffer_store_b128(v, r, off, 0, 16); }
DEV void wsst2(rsrc_t r, u32 off, u32x2 v) { __builtin_amdgcn_raw_buffer_store_b64(v, r, off, 0, 16); }
DEV u32 umax3(u32 a, u32 b, u32 c) { return max(max(a, b), c); }
DEV u32 vmax4(u32 m, u32x4 v) { return umax3(umax3(m, v[0], v[1]), v[2], v[3]); }
DEV u32 clampw(u32 v) { return min(v, 0xFFFFFFFEu); }
DEV u32x4 clamp4(u32x4 v) { return u32x4{clampw(v[0]), clampw(v[1]), clampw(v[2]), clampw(v[3])}; }
DEV u32 pk(float a, float b) { return __builtin_bit_cast(u32, __builtin_convertvector(f32x2{a, b}, bf16x2)); }
DEV float lo2f(u32 v) { return __uint_as_float(v << 16); }
DEV float hi2f(u32 v) { return __uint_as_float(v & 0xFFFF0000u); }
DEV float rbf(float f) { return lo2f(pk(f, 0.f)); }
DEV bf16x8 asbf(u32x4 v) { return __builtin_bit_cast(bf16x8, v); }
DEV f32x4 mfma16(u32x4 a, u32x4 b, f32x4 c) { return __builtin_amdgcn_mfma_f32_16x16x32_bf16(asbf(a), asbf(b), c, 0, 0, 0); }
DEV f32x16 mfma32(u32x4 a, u32x4 b, f32x16 c) { return __builtin_amdgcn_mfma_f32_32x32x16_bf16(asbf(a), asbf(b), c, 0, 0, 0); }
DEV u32x4 lds128(u32 a) { return *(LAS u32x4*)a; }
DEV void lds128w(u32 a, u32x4 v) { *(LAS u32x4*)a = v; }
DEV u32x2 ldstr(u32 a) { return __builtin_bit_cast(u32x2, __builtin_amdgcn_ds_read_tr16_b64_v4bf16((LAS bf16x4*)a)); }
DEV u32x4 ldstr2(u32 a0, u32 a1) { u32x2 x = ldstr(a0), y = ldstr(a1); return u32x4{x[0], x[1], y[0], y[1]}; }
DEV float ex2(float x) { return __builtin_amdgcn_exp2f(x); }

DEV float xor32_sum(float v) { auto r = __builtin_amdgcn_permlane32_swap(__float_as_uint(v), __float_as_uint(v), false, false); return __uint_as_float(r[0]) + __uint_as_float(r[1]); }
DEV float xor32_max(float v) { auto r = __builtin_amdgcn_permlane32_swap(__float_as_uint(v), __float_as_uint(v), false, false); return fmaxf(__uint_as_float(r[0]), __uint_as_float(r[1])); }
DEV float xor16_sum(float v) { auto r = __builtin_amdgcn_permlane16_swap(__float_as_uint(v), __float_as_uint(v), false, false); return __uint_as_float(r[0]) + __uint_as_float(r[1]); }
DEV float xor16_max(float v) { auto r = __builtin_amdgcn_permlane16_swap(__float_as_uint(v), __float_as_uint(v), false, false); return fmaxf(__uint_as_float(r[0]), __uint_as_float(r[1])); }
template <int CTRL> DEV float dpp(float v) { return __uint_as_float(__builtin_amdgcn_update_dpp(0u, __float_as_uint(v), CTRL, 0xF, 0xF, true)); }
constexpr int ROR8 = 0x128, ROR4 = 0x124, ROR2 = 0x122, ROR1 = 0x121;

#define STAMP(k)

#define POLL(N, V, CHECK, SLEEP, LOADS)                                  \
  _Pragma("unroll") for (int i = 0; i < N; ++i) pin(V[i]);               \
  _Pragma("nounroll") for (u32 sp = 0; sp < SPIN_CAP; ++sp) {            \
    u32 bad = 0;                                                         \
    _Pragma("unroll") for (int i = 0; i < N; ++i) bad |= CHECK;          \
    if (__builtin_amdgcn_ballot_w64(bad != 0) == 0) break;               \
    if (SLEEP) __builtin_amdgcn_s_sleep(SLEEP);                          \
    _Pragma("unroll") for (int i = 0; i < N; ++i) LOADS;                 \
    waitv<0>();                                                          \
    _Pragma("unroll") for (int i = 0; i < N; ++i) pin(V[i]);             \
  }
DEV void bar() { waitl(); __builtin_amdgcn_s_barrier(); CFENCE(); }
DEV bool allok(u32 bad) { return __builtin_amdgcn_ballot_w64(bad != 0) == 0; }
DEV u32 swz(int r) { return ((r & 3) << 2) | ((r >> 2) & 3); }

DEV void qpart(u32 wl, u32x4 xf, int lane, u32 sb, u32 sq) {
  const int g = lane >> 4, n = lane & 15, qq = n >> 2, p = lane & 3;
  f32x4 acc[4];
#pragma unroll
  for (int t = 0; t < 4; ++t) {
    const u32 a = wl + (8 * g + qq) * 128 + t * 32 + p * 8;
    acc[t] = mfma16(ldstr2(a, a + 512), xf, f32x4{0.f, 0.f, 0.f, 0.f});
  }
  CFENCE();
#pragma unroll
  for (int t = 0; t < 2; ++t) {
    const f32x4 u = acc[t], v = acc[2 + t];
    const bool lo = n < 8;
    *(LAS f32x4*)(wl + (n & 7) * sb + ((n >> 3) * 8 + t * 4 + g) * sq) = f32x4{lo ? u[0] : v[0], lo ? u[1] : v[1], lo ? u[2] : v[2], lo ? u[3] : v[3]};
  }
}
DEV u32x4 qsum(u32 qr, u32 tl, u32 th) {
  f32x4 q[8];
#pragma unroll
  for (int i = 0; i < 8; ++i) q[i] = *(LAS f32x4*)(qr + i * 4096);
  const f32x4 r = ((q[0] + q[1]) + (q[2] + q[3])) + ((q[4] + q[5]) + (q[6] + q[7]));
  return u32x4{pk(r[0], r[1]), pk(r[2], r[3]), tl, th};
}

__global__ __launch_bounds__(512, 2) void gm_kernel(Params P) {
  extern __shared__ __attribute__((aligned(16))) char smem[];
  const int tid = threadIdx.x, lane = tid & 63;
  const int w = __builtin_amdgcn_readfirstlane(tid >> 6);
  const int wg = blockIdx.x, b = wg & 7, idx = (wg >> 3) & 15;
  const int h = HMAP, jq = idx >> 2;
  const rsrc_t ws = __builtin_amdgcn_make_buffer_rsrc(P.ws, 0, WS_BYTES, 0x00020000);
  const int n = lane & 15, g = lane >> 4, qq = n >> 2, p = lane & 3;
  STAMP(0);
  (void)smem;
  if (wg < 128) {
    const int hkv = idx >> 1, half = idx & 1;
    u32x4 ep, xf;
    ldg_sc1(ep, 0u, P.ws);
    const size_t qoff = (size_t)((jq >> 1) * 256 + w * 32) * 8192 + h * 256 + (jq & 1) * 128;
#pragma unroll
    for (int s = 0; s < 4; ++s) dma_nt((u32)((lane >> 3) * 8192 + (lane & 7) * 16), P.wqa + qoff + s * 65536, L_WQA + w * 4096 + s * 1024);
    ldg(xf, (u32)((lane & 7) * 1024 + g * 16), P.x + (jq >> 1) * 512 + w * 64);
    bar();
    if (w < 2) {
#pragma unroll
      for (int k = 0; k < 16; ++k)
        dma_nt((u32)(g * 8192 + ((n ^ k) * 16)), P.wuk + (size_t)(jq * 128 + w * 64 + k * 4) * 8192 + h * 256, L_WUK + w * 16384 + k * 1024);
      waitv<16>();
    } else {
      waitv<0>();
    }
    pin(ep); pin(xf);
    const u64 epoch = (((u64)__builtin_amdgcn_readfirstlane(ep[1]) << 32) | (u32)__builtin_amdgcn_readfirstlane(ep[0])) + 0x0101010101010101ull;
    const u32 tl = (u32)epoch, th = (u32)(epoch >> 32);
    STAMP(1);
    qpart(L_WQA + w * 4096, xf, lane, 16, 128);
    bar();
    if ((w >> 1) == 1) wsst(ws, WS_QA + h * 8192 + (2 * (jq & 1) + (w & 1)) * 2048 + (n >> 3) * 1024 + g * 256 + (jq >> 1) * 128 + (lane & 7) * 16, qsum(L_WQA + (w & 1) * 1024 + lane * 16, tl, th));
    STAMP(2);
    const size_t kvoff = ((size_t)(b * 8 + hkv) * 4096 + 3072 + half * 512) * 256;
#define K_ISSUE(R0, CNT, WIN)                                                          \
  _Pragma("nounroll") for (int r = R0; r < R0 + CNT; ++r) {                            \
    const u32 pc = ((0x54107632u >> ((r >> 4) * 4)) & 7) * 16384 + (r & 15) * 1024;    \
    waitv<WIN - 1>();                                                                  \
    dma_nt((u32)((g * 256 + (n ^ g) * 16) ^ ((r & 3) * 64)), P.kc + kvoff + pc, pc);   \
  }
    constexpr int NR = (128 - 6 * NE) / 8;
    if (w < 2) {
      u32x4 uk[16], v[9];
      waitv<0>();
      STAMP(10);
#pragma unroll
      for (int t = 0; t < 4; ++t)
#pragma unroll
        for (int s = 0; s < 4; ++s) uk[t * 4 + s] = lds128(L_WUK + w * 16384 + (4 * n + t) * 256 + (((4 * s + g) ^ n) * 16));
      v[0][2] = ~tl;
      POLL(9, v, (v[i][2] ^ tl) | (v[i][3] ^ th), 0,
           (i < 8 ? ldg_sc1(v[i], (u32)(lane * 16), P.ws + WS_QA + h * 8192 + i * 1024)
                  : ldg_sc1(v[i], (u32)((lane & 7) * 16), P.ws + WS_FQ + (h >> 1) * 128)))
      STAMP(3);
      f32x4 d[4];
#pragma unroll
      for (int t = 0; t < 4; ++t) {
        d[t] = f32x4{0.f, 0.f, 0.f, 0.f};
#pragma unroll
        for (int s = 0; s < 4; ++s) d[t] = mfma16(u32x4{v[2 * s][0], v[2 * s][1], v[2 * s + 1][0], v[2 * s + 1][1]}, uk[t * 4 + s], d[t]);
      }
#pragma unroll
      for (int r = 0; r < 2; ++r) {
        float e[4];
#pragma unroll
        for (int t = 0; t < 4; ++t) {
          auto s2 = __builtin_amdgcn_permlane32_swap(__float_as_uint(d[t][r]), __float_as_uint(d[t][2 + r]), false, false);
          e[t] = (__uint_as_float(s2[0]) + __uint_as_float(s2[1])) * SCL;
        }
        const int bb = 4 * (g & 1) + 2 * (g >> 1) + r;
        wsst2(ws, WS_QL + (bb * 32 + h) * 1024 + jq * 256 + w * 128 + n * 8, u32x2{clampw(pk(e[0], e[1])), clampw(pk(e[2], e[3]))});
      }
    } else {
      K_ISSUE((w - 2) * NE, NE, WE)
    }
    bar();
    STAMP(4);
#pragma unroll
    for (int s = 0; s < 4; ++s) dma_nt((u32)((lane >> 3) * 8192 + (lane & 7) * 16), P.wqb + qoff + s * 65536, L_WQB + w * 4096 + s * 1024);
#pragma unroll
    for (int k = 0; k < 4; ++k) wsst(ws, WS_AP + (h * 4 + jq) * 32768 + (w * 4 + k) * 1024 + lane * 16, u32x4{SENT, SENT, SENT, SENT});
    u32x4 vv[16], pf[2];
    float mx, l;
    f32x4 o[8];
#define V_LOAD ldg_nt(vv[i], (u32)(g * 1024 + n * 16), P.vc + kvoff + w * 16384 + (32 * (i >> 3) + 16 * ((i >> 2) & 1) + (i & 3)) * 256)
#define V_ISSUE(I0, I1)                                                                                                       \
  _Pragma("unroll") for (int i = I0; i < I1; ++i) {                                                                           \
    waitv<WB - 1>();                                                                                                          \
    V_LOAD;                                                                                                                   \
  }
#define PV_STEP(gm)                                                                                   \
  _Pragma("unroll") for (int k = 0; k < 8; ++k) pin(vv[gm * 8 + k]);                                  \
  _Pragma("unroll") for (int c = 0; c < 4; ++c) {                                                     \
    u32x4 t0, t1;                                                                                     \
    _Pragma("unroll") for (int jp = 0; jp < 4; ++jp) {                                                \
      const u32 a = vv[gm * 8 + 2 * jp][c], bq = vv[gm * 8 + 2 * jp + 1][c];                          \
      t0[jp] = __builtin_amdgcn_perm(bq, a, 0x05040100);                                              \
      t1[jp] = __builtin_amdgcn_perm(bq, a, 0x07060302);                                              \
    }                                                                                                 \
    o[2 * c] = mfma16(pf[gm], t0, gm ? o[2 * c] : f32x4{0.f, 0.f, 0.f, 0.f});                         \
    o[2 * c + 1] = mfma16(pf[gm], t1, gm ? o[2 * c + 1] : f32x4{0.f, 0.f, 0.f, 0.f});                 \
  }
    waitv<0>();
    qpart(L_WQB + w * 4096, xf, lane, 256, 16);
    bar();
    if ((w >> 1) == 1) wsst(ws, WS_QB + ((((w & 1) * 4 + g) * 2 + (jq >> 1)) * 32 + h) * 512 + (jq & 1) * 256 + n * 16, qsum(L_WQB + (w & 1) * 1024 + lane * 16, tl, th));
    if (w == 4 && lane == 0) wsst(ws, WS_FP + (jq * 32 + h) * 16, u32x4{0, 0, tl, th});
    STAMP(5);
    __builtin_amdgcn_s_sleep(BDLY);
    K_ISSUE(6 * NE + w * NR, NR, WB)
    if (w == 0) {
      u32x4 v[4];
      v[0][2] = ~tl;
      POLL(4, v, (v[i][2] ^ tl) | (v[i][3] ^ th), 0, ldg_sc1(v[i], (u32)(lane * 16), P.ws + WS_QB + ((b * 2 + (i >> 1)) * 32 + hkv * 4) * 512 + (i & 1) * 1024))
#pragma unroll
      for (int i = 0; i < 2; ++i)
        *(LAS u32x2*)(L_QBS + i * 512 + lane * 8) = u32x2{pk(lo2f(v[i][0]) + lo2f(v[2 + i][0]), hi2f(v[i][0]) + hi2f(v[2 + i][0])), pk(lo2f(v[i][1]) + lo2f(v[2 + i][1]), hi2f(v[i][1]) + hi2f(v[2 + i][1]))};
    }
    V_ISSUE(0, WB)
      {
        waitv<WB>();
#pragma unroll
        for (int i = WB; i < WB + VX; ++i) V_LOAD;
        STAMP(6);
        bar();
        u32x4 qb[4];
#pragma unroll
        for (int s = 0; s < 4; ++s) qb[s] = lds128(L_QBS + (n & 3) * 256 + s * 64 + g * 16);
        f32x4 sc[4];
        mx = -3.0e38f;
#pragma unroll
        for (int t = 0; t < 4; ++t) {
          sc[t] = f32x4{0.f, 0.f, 0.f, 0.f};
#pragma unroll
          for (int s = 0; s < 4; ++s) sc[t] = mfma16(lds128(w * 16384 + (t * 16 + n) * 256 + (((4 * s + g) ^ n) * 16)), qb[s], sc[t]);
          sc[t] *= SCL;
          mx = fmaxf(fmaxf(mx, fmaxf(sc[t][0], sc[t][1])), fmaxf(sc[t][2], sc[t][3]));
        }
        mx = xor32_max(xor16_max(mx));
        l = 0.f;
#pragma unroll
        for (int t = 0; t < 4; ++t)
#pragma unroll
          for (int e = 0; e < 4; ++e) { sc[t][e] = ex2(sc[t][e] - mx); l += sc[t][e]; }
        l = xor32_sum(xor16_sum(l));
#pragma unroll
        for (int gm = 0; gm < 2; ++gm)
          pf[gm] = u32x4{pk(sc[2 * gm][0], sc[2 * gm][1]), pk(sc[2 * gm][2], sc[2 * gm][3]), pk(sc[2 * gm + 1][0], sc[2 * gm + 1][1]), pk(sc[2 * gm + 1][2], sc[2 * gm + 1][3])};
        if (lane < 4) *(LAS f32x2*)(L_BML + lane * 64 + w * 8) = f32x2{mx, l};
        STAMP(7);
      }
    V_ISSUE(WB + VX, 12)
    bar();
    const int hh = 2 * (w & 1) + (lane >> 5), un = lane & 31;
    float me[8], mlo;
    {
      f32x4 ml[4];
#pragma unroll
      for (int i = 0; i < 4; ++i) ml[i] = *(LAS f32x4*)(L_BML + hh * 64 + i * 16);
      float M = ml[0][0];
#pragma unroll
      for (int i = 1; i < 8; ++i) M = fmaxf(M, ml[i >> 1][(i & 1) * 2]);
      float L = 0.f;
#pragma unroll
      for (int i = 0; i < 8; ++i) {
        me[i] = ex2(ml[i >> 1][(i & 1) * 2] - M);
        L += me[i] * ml[i >> 1][(i & 1) * 2 + 1];
      }
      mlo = (un & 1) ? L : M;
    }
    V_ISSUE(12, 16)
    waitv<8>();
    PV_STEP(0)
    waitv<0>();
    STAMP(8);
    PV_STEP(1)
    if (lane < 16) {
#pragma unroll
      for (int k = 0; k < 4; ++k) {
        *(LAS f32x4*)(L_BM + w * 2048 + k * 512 + n * 32) = f32x4{o[0][k], o[1][k], o[2][k], o[3][k]};
        *(LAS f32x4*)(L_BM + w * 2048 + k * 512 + n * 32 + 16) = f32x4{o[4][k], o[5][k], o[6][k], o[7][k]};
      }
    }
    bar();
    if (w < 2) {
      f32x4 r = {0.f, 0.f, 0.f, 0.f}, r1 = r;
#pragma unroll
      for (int i = 0; i < 4; ++i) {
        r += me[i] * *(LAS f32x4*)(L_BM + i * 2048 + hh * 512 + un * 16);
        r1 += me[4 + i] * *(LAS f32x4*)(L_BM + (4 + i) * 2048 + hh * 512 + un * 16);
      }
      r += r1;
      wsst(ws, WS_BP + (((hkv * 4 + hh) * 2 + half) * 8 + b) * 512 + un * 16, u32x4{pk(r[0], r[1]), pk(r[2], r[3]), tl, __float_as_uint(mlo)});
    }
    STAMP(9);
  } else {
    const char* cb = P.ckv + ((size_t)b * 4096 + idx * 256 + w) * 1024;
#define CROW(i) (cb + (i) * 8192)
    u32x4 ep, gx[2], sg[16], v[4], fl[1];
    ldg_sc1(ep, 0u, P.ws);
    if (w == 7) {
#pragma unroll
      for (int k = 0; k < 2; ++k) wsst(ws, WS_QL + (b * 32 + 2 * idx + k) * 1024 + lane * 16, u32x4{SENT, SENT, SENT, SENT});
    }
    ldg(gx[0], (u32)(lane * 16), P.gw);
    ldg(gx[1], (u32)(lane * 16), P.x + w * 1024);
#define CKV_ISSUE(I0, I1)                                                                                   \
  _Pragma("unroll") for (int i = I0; i < I1; ++i) {                                                         \
    if (i >= WA) waitv<WA - 1>();                                                                           \
    dma_nt((u32)((lane ^ swz(i * 8 + w)) * 16), CROW(i), (u32)((i * 8 + w) * 1024));        \
  }
    CKV_ISSUE(0, WA + 1)
    pin(ep); pin(gx[0]); pin(gx[1]);
    const u64 epoch = (((u64)__builtin_amdgcn_readfirstlane(ep[1]) << 32) | (u32)__builtin_amdgcn_readfirstlane(ep[0])) + 0x0101010101010101ull;
    const u32 tl = (u32)epoch, th = (u32)(epoch >> 32);
    if (w == 7 && lane == 0) wsst(ws, WS_FQ + (idx * 8 + b) * 16, u32x4{0, 0, tl, th});
    CKV_ISSUE(WA + 1, 16)
#define FQ_LOAD ldg_sc1(fl[0], (u32)((lane & 1) * 128), P.ws + WS_FQ + (2 * w * 8 + b) * 16)
    FQ_LOAD;
    bar();
    STAMP(1);
    float gate;
    {
      float a = 0.f;
#pragma unroll
      for (int e = 0; e < 4; ++e) a += rbf(lo2f(gx[1][e]) * lo2f(gx[0][e])) + rbf(hi2f(gx[1][e]) * hi2f(gx[0][e]));
      a += dpp<ROR1>(a); a += dpp<ROR2>(a); a += dpp<ROR4>(a); a += dpp<ROR8>(a);
      a = xor32_sum(xor16_sum(a));
      gate = rbf(1.f / (1.f + __expf(-rbf(a))));
    }
#define QL_LOAD ldg_sc1(v[i], (u32)(lane * 16), P.ws + WS_QL + (b * 32 + 4 * w + i) * 1024)
#define SG_LOAD ldg_nt(sg[i], (u32)((lane ^ swz(i * 8 + w)) * 16), CROW(16 + i))
#pragma unroll
    for (int i = 0; i < NPRE; ++i) { waitv<WA - 1>(); SG_LOAD; }
    waitv<NPRE>();
    POLL(1, fl, (fl[i][2] ^ tl) | (fl[i][3] ^ th), 1, FQ_LOAD)
    STAMP(11);
#pragma unroll
    for (int i = 0; i < 4; ++i) QL_LOAD;
#pragma unroll
    for (int i = NPRE; i < NPRE + NMID; ++i) { waitv<WA + 3>(); SG_LOAD; }
    waitv<NMID>();
    POLL(4, v, (u32)(vmax4(0, v[i]) == SENT), 1, rel_sc1(v[i], vmax4(0, v[i]) == SENT, (u32)(lane * 16), P.ws + WS_QL + (b * 32 + 4 * w + i) * 1024))
    STAMP(2);
#pragma unroll
    for (int i = NPRE + NMID; i < NPRE + NMID + NSTEP; ++i) SG_LOAD;
#pragma unroll
    for (int i = 0; i < 4; ++i) lds128w(L_X + (4 * w + i) * 1024 + ((lane ^ swz(4 * w + i)) * 16), v[i]);
    bar();
    const int r32 = lane & 31, hf = lane >> 5, kg = w & 3, hh2 = w >> 2;
    const u32 fb = n * 1024 + 16 * ((4 * g) ^ swz(n));
    u32x4 qf[16];
#pragma unroll
    for (int ks = 0; ks < 16; ++ks) qf[ks] = lds128(L_X + hh2 * 16384 + (fb ^ (16 * (ks & 3))) + 256 * (ks >> 2));
    f32x16 o[2];
    float m_run = -3.0e38f, m_sc = -3.0e38f, l_run = 0.f;
#pragma unroll
    for (int ph = 0; ph < 2; ++ph) {
      f32x4 sc[2] = {{0.f, 0.f, 0.f, 0.f}, {0.f, 0.f, 0.f, 0.f}};
#pragma unroll
      for (int ks = 0; ks < 16; ++ks)
#pragma unroll
        for (int t = 0; t < 2; ++t) sc[t] = mfma16(lds128(kg * 32768 + t * 16384 + (fb ^ (16 * (ks & 3))) + 256 * (ks >> 2)), qf[ks], sc[t]);
#define AP_OFF(k) (WS_AP + ((((8 * k + (lane >> 3)) * 4 + (w >> 1)) * 8 + b) * 16 + idx) * 256 + (w & 1) * 128 + (lane & 7) * 16)
      if (ph == 0) {
#pragma unroll
        for (int i = NPRE + NMID + NSTEP; i < NPRE + NMID + 2 * NSTEP && i < 16; ++i) SG_LOAD;
      }
      float mx = fmaxf(fmaxf(fmaxf(sc[0][0], sc[0][1]), fmaxf(sc[0][2], sc[0][3])), fmaxf(fmaxf(sc[1][0], sc[1][1]), fmaxf(sc[1][2], sc[1][3])));
      mx = xor32_max(xor16_max(mx));
      if (ph == 0) bar();
      if (lane < 16) *(LAS float*)(L_MAXB + (hh2 * 16 + n) * 16 + kg * 4) = mx;
      bar();
      const f32x4 ma = *(LAS f32x4*)(L_MAXB + (hh2 * 16 + n) * 16), mb = *(LAS f32x4*)(L_MAXB + r32 * 16);
      mx = fmaxf(fmaxf(fmaxf(ma[0], ma[1]), fmaxf(ma[2], ma[3])), m_sc);
      float l = 0.f;
#pragma unroll
      for (int t = 0; t < 2; ++t) {
#pragma unroll
        for (int i = 0; i < 4; ++i) { sc[t][i] = ex2(sc[t][i] - mx); l += sc[t][i]; }
        *(LAS u32x2*)(L_P + (2 * kg + t) * 1024 + ((g & 1) * 32 + hh2 * 16 + n) * 16 + (g >> 1) * 8) = u32x2{pk(sc[t][0], sc[t][1]), pk(sc[t][2], sc[t][3])};
      }
      l_run = l_run * ex2(m_sc - mx) + l;
      m_sc = mx;
      if (ph == 1) {
        const float ls = xor32_sum(xor16_sum(l_run));
        if (lane < 16) *(LAS float*)(L_SUMB + (hh2 * 16 + n) * 16 + kg * 4) = ls;
      }
      const float mo = fmaxf(fmaxf(fmaxf(mb[0], mb[1]), fmaxf(mb[2], mb[3])), m_run);
      const float alpha = ex2(m_run - mo);
      m_run = mo;
#pragma unroll
      for (int ct = 0; ct < 2; ++ct)
#pragma unroll
        for (int i = 0; i < 16; ++i) o[ct][i] = ph ? o[ct][i] * alpha : 0.f;
      bar();
      STAMP(3 + 3 * ph);
      if (ph == 0) {
#pragma unroll
        for (int i = NPRE + NMID + 2 * NSTEP; i < NPRE + NMID + 3 * NSTEP && i < 16; ++i) SG_LOAD;
      }
      if (ph == 1) {
#pragma unroll
        for (int i = 4; i < 8 + 4 * WOBR; ++i) ldg_nt(sg[i], (u32)(g * 1024 + n * 16), (i < 8 ? P.woa : P.wob) + (size_t)(h * 128 + (4 * w + (i & 3)) * 4) * 1024 + jq * 256);
      }
      if (ph == 1 && w == 0) {
        const f32x4 a = *(LAS f32x4*)(L_SUMB + r32 * 16);
        const float lsum = (a[0] + a[1]) + (a[2] + a[3]);
        if (lane < 32) wsst(ws, WS_ML + ((b * 16 + idx) * 32 + lane) * 16, u32x4{__float_as_uint(m_run), __float_as_uint(lsum), tl, th});
      }
      {
        const int blk = g & 1;
        const u32 a0 = ((4 * hf + qq) * 1024 + (w >> 1) * 256 + 16 * ((2 * blk + (p >> 1)) ^ ((qq << 2) | hf)) + 8 * (p & 1)) ^ (128 * (w & 1));
        const u32 a1 = (a0 ^ 32) + 8192;
#pragma unroll
        for (int ks = 0; ks < 8; ++ks) {
          if (ph == 0 && ks == 4) {
#pragma unroll
            for (int i = NPRE + NMID + 3 * NSTEP; i < 16; ++i) SG_LOAD;
          }
          const u32x4 pf = lds128(L_P + ks * 1024 + lane * 16);
#pragma unroll
          for (int ct = 0; ct < 2; ++ct) o[ct] = mfma32(ldstr2((a0 ^ (64 * ct)) + 16384 * ks, (a1 ^ (64 * ct)) + 16384 * ks), pf, o[ct]);
        }
      }
      if (ph == 0) {
        bar();
        STAMP(4);
        waitv<0>();
#pragma unroll
        for (int i = 0; i < 16; ++i) { pin(sg[i]); lds128w((i * 8 + w) * 1024 + lane * 16, sg[i]); }
#define FP_LOAD ldg_sc1(fl[0], (u32)(r32 * 16), P.ws + WS_FP + (w >> 1) * 512)
        waitl();
        FP_LOAD;
#pragma unroll
        for (int i = 0; i < 4; ++i) ldg_nt(sg[i], (u32)(g * 8192 + n * 16), P.wuv + (size_t)(jq * 128 + (4 * w + i) * 4) * 8192 + h * 256);
        bar();
        STAMP(5);
      }
    }
    bar();
    STAMP(7);
#pragma unroll
    for (int ct = 0; ct < 2; ++ct)
#pragma unroll
      for (int uu = 0; uu < 2; ++uu)
        lds128w(L_X + w * 4096 + r32 * 128 + (((ct * 4 + hf * 2 + uu) ^ (r32 & 7)) * 16),
                clamp4(u32x4{pk(o[ct][8 * uu], o[ct][8 * uu + 1]), pk(o[ct][8 * uu + 2], o[ct][8 * uu + 3]), pk(o[ct][8 * uu + 4], o[ct][8 * uu + 5]), pk(o[ct][8 * uu + 6], o[ct][8 * uu + 7])}));
    if (lane == 0) *(LAS float*)(L_CL + 4096 + w * 4) = gate;
    waitv<8 + 4 * WOBR>();
    POLL(1, fl, (fl[i][2] ^ tl) | (fl[i][3] ^ th), 1, FP_LOAD)
    waitl();
#pragma unroll
    for (int k = 0; k < 4; ++k) {
      const int hh = 8 * k + (lane >> 3);
      wsst(ws, AP_OFF(k), lds128(L_X + w * 4096 + hh * 128 + (((lane & 7) ^ (hh & 7)) * 16)));
    }
    STAMP(8);
    u32x4 wu[4], wa[4], wb[4];
    {
      u32x4 mlv[1];
      ldg_sc1(mlv[0], (u32)(n * 512), P.ws + WS_ML + (w * 512 + h) * 16);
      waitv<5>();
#pragma unroll
      for (int i = 0; i < 8 + 4 * WOBR; ++i) { pin(sg[i]); lds128w((i >> 2) * 32768 + (4 * w + (i & 3)) * 1024 + lane * 16, sg[i]); }
      bar();
#pragma unroll
      for (int i = 0; i < 4; ++i) {
        const u32 a = L_WUV + (32 * i + 16 * (g & 1) + 4 * (g >> 1) + qq) * 256 + w * 32 + p * 8;
        wu[i] = ldstr2(a, a + 2048);
        const u32 a2 = L_WOA + (32 * i + 8 * g + qq) * 256 + w * 32 + p * 8;
        wa[i] = ldstr2(a2, a2 + 1024);

      }
      waitv<0>();
      POLL(1, mlv, (mlv[i][2] ^ tl) | (mlv[i][3] ^ th), 2, ldg_sc1(mlv[i], (u32)(n * 512), P.ws + WS_ML + (w * 512 + h) * 16))
      const u32x4 ml = mlv[0];
#pragma unroll
      for (int i = 0; i < 4; ++i) ldg_sc1(v[i], (u32)(lane * 16), P.ws + WS_AP + ((h * 4 + jq) * 8 + w) * 4096 + i * 1024);
      const float ms = __uint_as_float(ml[0]);
      float M = ms;
      M = fmaxf(M, dpp<ROR1>(M)); M = fmaxf(M, dpp<ROR2>(M)); M = fmaxf(M, dpp<ROR4>(M)); M = fmaxf(M, dpp<ROR8>(M));
      const float e = ex2(ms - M);
      float L = e * __uint_as_float(ml[1]);
      L += dpp<ROR1>(L); L += dpp<ROR2>(L); L += dpp<ROR4>(L); L += dpp<ROR8>(L);
      const float wgt = e * __builtin_amdgcn_rcpf(L);
      float wv[4], acc[8];
#pragma unroll
      for (int i = 0; i < 4; ++i) wv[i] = __uint_as_float(__builtin_amdgcn_ds_bpermute((4 * i + g) * 4, __float_as_uint(wgt)));
      waitv<0>();
      POLL(4, v, (u32)(vmax4(0, v[i]) == SENT), 1, rel_sc1(v[i], vmax4(0, v[i]) == SENT, (u32)(lane * 16), P.ws + WS_AP + ((h * 4 + jq) * 8 + w) * 4096 + i * 1024))
      STAMP(9);
#pragma unroll
      for (int i = 0; i < 4 - 4 * WOBR; ++i) dma_nt((u32)(g * 1024 + n * 16), P.wob + (size_t)(h * 128 + (4 * w + i) * 4) * 1024 + jq * 256, L_WOB + (4 * w + i) * 1024);
#pragma unroll
      for (int i = 0; i < 4; ++i) {
#pragma unroll
        for (int e2 = 0; e2 < 4; ++e2) {
          acc[2 * e2] = (i ? acc[2 * e2] : 0.f) + wv[i] * lo2f(v[i][e2]);
          acc[2 * e2 + 1] = (i ? acc[2 * e2 + 1] : 0.f) + wv[i] * hi2f(v[i][e2]);
        }
      }
      float r4[4], r2[2];
#pragma unroll
      for (int e2 = 0; e2 < 4; ++e2) {
        auto r = __builtin_amdgcn_permlane32_swap(__float_as_uint(acc[e2]), __float_as_uint(acc[4 + e2]), false, false);
        r4[e2] = __uint_as_float(r[0]) + __uint_as_float(r[1]);
      }
#pragma unroll
      for (int e2 = 0; e2 < 2; ++e2) {
        auto r = __builtin_amdgcn_permlane16_swap(__float_as_uint(r4[e2]), __float_as_uint(r4[2 + e2]), false, false);
        r2[e2] = __uint_as_float(r[0]) + __uint_as_float(r[1]);
      }
      *(LAS u32*)(L_CL + (n * 8 + w) * 16 + g * 4) = pk(r2[0], r2[1]);
    }
    bar();
    STAMP(10);
    {
      f32x4 d = {0.f, 0.f, 0.f, 0.f};
#pragma unroll
      for (int ks = 0; ks < 4; ++ks) {
        d = mfma16(wu[ks], lds128(L_CL + ((ks * 4 + g) * 8 + (n & 7)) * 16), d);
      }
      if (n < 8) wsst(ws, WS_CT + (h * 4 + jq) * 4096 + (w * 4 + g) * 128 + n * 16, u32x4{pk(d[0], d[1]), pk(d[2], d[3]), tl, th});
    }
    {
      const f32x4 gg = *(LAS f32x4*)(L_CL + 4096 + (g & 1) * 16);
#define CB_ADDR (u32)(hf * 4096 + r32 * 16), P.ws + (u32)__builtin_amdgcn_readfirstlane((i < 2 ? WS_CT + (h * 4 + 2 * i) * 4096 : WS_BP + h * 8192) + w * 512)
#define CB_BAD ((v[i][2] ^ tl) | (i < 2 ? v[i][3] ^ th : 0u))
      waitv<0>();
#pragma unroll
      for (int i = 0; i < 3; ++i) ldg_sc1(v[i], CB_ADDR);
      bar();
#pragma unroll
      for (int i = 0; i < 4; ++i) {
        const u32 a = L_WOB + (32 * i + 8 * g + qq) * 256 + w * 32 + p * 8;
        wb[i] = ldstr2(a, a + 1024);
      }
      waitv<0>();
      POLL(3, v, CB_BAD, 0, rel_sc1(v[i], CB_BAD != 0, CB_ADDR))
      STAMP(12);
      {
        auto s0 = __builtin_amdgcn_permlane32_swap(__float_as_uint(lo2f(v[0][0]) + lo2f(v[1][0])), __float_as_uint(lo2f(v[0][1]) + lo2f(v[1][1])), false, false);
        auto s1 = __builtin_amdgcn_permlane32_swap(__float_as_uint(hi2f(v[0][0]) + hi2f(v[1][0])), __float_as_uint(hi2f(v[0][1]) + hi2f(v[1][1])), false, false);
        *(LAS u32*)(L_CL + 8192 + ((2 * w + (r32 >> 4)) * 8 + (r32 & 7)) * 16 + ((r32 >> 3) & 1) * 8 + hf * 4) =
            pk(__uint_as_float(s0[0]) + __uint_as_float(s0[1]), __uint_as_float(s1[0]) + __uint_as_float(s1[1]));
      }
      {
        const float x = __uint_as_float(v[2][3]), y = dpp<0x141>(x);
        const float m0 = (lane & 1) ? y : x, l0 = (lane & 1) ? x : y;
        const float e = ex2(m0 - xor32_max(m0));
        const float wgt = e * rbf(1.f - gate) * __builtin_amdgcn_rcpf(xor32_sum(e * l0));
        auto s0 = __builtin_amdgcn_permlane32_swap(__float_as_uint(wgt * lo2f(v[2][0])), __float_as_uint(wgt * lo2f(v[2][1])), false, false);
        auto s1 = __builtin_amdgcn_permlane32_swap(__float_as_uint(wgt * hi2f(v[2][0])), __float_as_uint(wgt * hi2f(v[2][1])), false, false);
        *(LAS u32*)(L_CL + 12288 + ((r32 >> 1) * 8 + w) * 16 + (r32 & 1) * 8 + hf * 4) =
            pk(__uint_as_float(s0[0]) + __uint_as_float(s0[1]), __uint_as_float(s1[0]) + __uint_as_float(s1[1]));
      }
      bar();
      STAMP(13);
      f32x4 da = {0.f, 0.f, 0.f, 0.f}, db = {0.f, 0.f, 0.f, 0.f};
#pragma unroll
      for (int i = 0; i < 4; ++i) {
        da = mfma16(lds128(L_CL + 8192 + ((4 * i + g) * 8 + (n & 7)) * 16), wa[i], da);
        db = mfma16(lds128(L_CL + 12288 + ((4 * i + g) * 8 + (n & 7)) * 16), wb[i], db);
      }
      da = da * gg + db;
      u32x4 ov;
#pragma unroll
      for (int r = 0; r < 4; ++r) ov[r] = (__float_as_uint(da[r]) & 0xFFFFFF00u) | ((tl >> (8 * r)) & 0xFFu);
      if (lane < 32) wsst(ws, WS_OP + ((jq * 32 + w * 4 + (lane >> 3)) * 32 + h) * 128 + (lane & 7) * 16, ov);
      STAMP(14);
      if (w == 0) {
        const int red = wg - 128;
        waitv<0>();
        v[0][0] = ~tl;
        POLL(4, v, __builtin_amdgcn_perm(__builtin_amdgcn_perm(v[i][3], v[i][2], 0x0c0c0400), __builtin_amdgcn_perm(v[i][1], v[i][0], 0x0c0c0400), 0x05040100) ^ tl, 0,
             ldg_sc1(v[i], (u32)(lane * 16), P.ws + WS_OP + red * 4096 + i * 1024))
        float r[4];
#pragma unroll
        for (int e2 = 0; e2 < 4; ++e2) {
          float x = (__uint_as_float(v[0][e2] & 0xFFFFFF00u) + __uint_as_float(v[1][e2] & 0xFFFFFF00u)) + (__uint_as_float(v[2][e2] & 0xFFFFFF00u) + __uint_as_float(v[3][e2] & 0xFFFFFF00u));
          x += dpp<ROR8>(x);
          r[e2] = xor32_sum(xor16_sum(x));
        }
        const float rv = g == 0 ? r[0] : g == 1 ? r[1] : g == 2 ? r[2] : r[3];
        const int L = red & 7, col = (red >> 3) * 32 + 16 * (L >> 2) + 8 * (L & 1) + n, bb = 4 * ((L >> 1) & 1) + g;
        if (n < 8) *(unsigned short*)(P.out + (bb * 512 + col) * 2) = (unsigned short)pk(rv, 0.f);
        if (wg == 128 && lane == 0) wsst2(ws, WS_EPOCH, u32x2{tl, th});
        STAMP(15);
      }
    }
  }
}

extern "C" void kernel_launch(void* const* d_in, const int* in_sizes, int n_in, void* d_out, int out_size, void* d_ws, size_t ws_size, hipStream_t stream) {
  Params p;
  p.x = (const char*)d_in[0]; p.ckv = (const char*)d_in[1]; p.wqa = (const char*)d_in[2]; p.wuk = (const char*)d_in[3];
  p.wuv = (const char*)d_in[4]; p.woa = (const char*)d_in[5]; p.kc = (const char*)d_in[7]; p.vc = (const char*)d_in[8];
  p.wqb = (const char*)d_in[9]; p.wob = (const char*)d_in[10]; p.gw = (const char*)d_in[11];
  p.out = (char*)d_out; p.ws = (char*)d_ws;
  (void)hipFuncSetAttribute((const void*)gm_kernel, hipFuncAttributeMaxDynamicSharedMemorySize, LDS_BYTES);
  hipLaunchKernelGGL(gm_kernel, dim3(256), dim3(512), LDS_BYTES, stream, p);
}
```

```cpp
#pragma clang diagnostic ignored "-Wint-to-pointer-cast"
#include <hip/hip_runtime.h>
#include <hip/hip_bf16.h>
#include <stdint.h>

typedef unsigned int u32;
typedef unsigned long long u64;
typedef u32 u32x4 __attribute__((ext_vector_type(4)));
typedef u32 u32x2 __attribute__((ext_vector_type(2)));
typedef float f32x4 __attribute__((ext_vector_type(4)));
typedef float f32x2 __attribute__((ext_vector_type(2)));
typedef float f32x16 __attribute__((ext_vector_type(16)));
typedef __bf16 bf16x8 __attribute__((ext_vector_type(8)));
typedef __bf16 bf16x4 __attribute__((ext_vector_type(4)));
typedef __bf16 bf16x2 __attribute__((ext_vector_type(2)));
#define LAS __attribute__((address_space(3)))
#define DEV __device__ __forceinline__

#define HMAP (b + 8 * (idx & 3))
#define WA 4
#define NPRE 8
#define NSTEP 4
#define NMID 0
#define WB 4
#define ADLY 16
#define BDLY 16
#define WOAP 0
#define PFM 0
#define VX 0
constexpr u32 WS_EPOCH = 0;
constexpr u32 WS_FQ   = 4096;
constexpr u32 WS_FP   = 8192;
constexpr u32 WS_QA   = 16384;
constexpr u32 WS_QB   = WS_QA + 262144;
constexpr u32 WS_QL   = WS_QB + 262144;
constexpr u32 WS_ML   = WS_QL + 262144;
constexpr u32 WS_CT   = WS_ML + 65536;
constexpr u32 WS_BP   = WS_CT + 524288;
constexpr u32 WS_OP   = WS_BP + 262144;
constexpr u32 WS_AP   = WS_OP + 524288;
constexpr u32 WS_BYTES = WS_AP + 4194304;
constexpr u32 WS_DBG = 16u << 20;

constexpr u32 L_X = 131072;
constexpr u32 L_P = L_X, L_MAXB = L_X + 8192, L_SUMB = L_X + 8704;
constexpr u32 L_WUV = 0, L_WOA = 32768, L_WOB = 65536, L_CL = 98304;
constexpr u32 L_WUK = 0, L_WQA = 65536, L_WQB = L_X;
constexpr u32 L_BM = L_X + 16384, L_BML = L_X + 3072, L_QBS = L_X + 2048;
constexpr u32 LDS_BYTES = 163840;
constexpr u32 SENT = 0xFFFFFFFFu;
#define DMA_Q dma_nt
constexpr u32 SPIN_CAP = 1u << 20;
constexpr float SCL = 0.08838834764831845f * 1.4426950408889634f;

struct Params {
  const char *x, *ckv, *wqa, *wuk, *wuv, *woa, *kc, *vc, *wqb, *wob, *gw;
  char *out, *ws;
};

template <int N> DEV void waitv() { asm volatile("s_waitcnt vmcnt(%0)" ::"i"(N) : "memory"); }
DEV void waitl() { asm volatile("s_waitcnt lgkmcnt(0)" ::: "memory"); }
DEV void ldg_nt(u32x4& r, u32 voff, const char* sb) { asm volatile("global_load_dwordx4 %0, %1, %2 nt" : "=v"(r) : "v"(voff), "s"(sb) : "memory"); }
DEV void ldg(u32x4& r, u32 voff, const char* sb) { asm volatile("global_load_dwordx4 %0, %1, %2" : "=v"(r) : "v"(voff), "s"(sb) : "memory"); }
DEV void ldg_sc1(u32x4& r, u32 voff, const char* sb) { asm volatile("global_load_dwordx4 %0, %1, %2 sc1" : "=v"(r) : "v"(voff), "s"(sb) : "memory"); }
DEV void rel_sc1(u32x4& r, bool miss, u32 voff, const char* sb) {
  asm volatile("s_mov_b64 exec, %3\n\tglobal_load_dwordx4 %0, %1, %2 sc1\n\ts_mov_b64 exec, -1" : "+v"(r) : "v"(voff), "s"(sb), "s"(__builtin_amdgcn_ballot_w64(miss)) : "memory");
}
DEV void dma_nt(u32 voff, const char* sb, u32 lds) {
  u32 keep;
  asm volatile("s_mov_b32 %0, m0\n\ts_mov_b32 m0, %3\n\ts_nop 0\n\tglobal_load_lds_dwordx4 %1, %2 nt\n\ts_mov_b32 m0, %0" : "=&s"(keep) : "v"(voff), "s"(sb), "s"(lds) : "memory");
}
DEV void dma(u32 voff, const char* sb, u32 lds) {
  u32 keep;
  asm volatile("s_mov_b32 %0, m0\n\ts_mov_b32 m0, %3\n\ts_nop 0\n\tglobal_load_lds_dwordx4 %1, %2\n\ts_mov_b32 m0, %0" : "=&s"(keep) : "v"(voff), "s"(sb), "s"(lds) : "memory");
}
DEV void pin(u32x4& r) { asm volatile("" : "+v"(r)); }
#define CFENCE() asm volatile("" ::: "memory")

typedef __amdgpu_buffer_rsrc_t rsrc_t;
DEV u32x4 wsld(rsrc_t r, u32 off) { return __builtin_amdgcn_raw_buffer_load_b128(r, off, 0, 16); }
DEV u32x2 wsld2(rsrc_t r, u32 off) { return __builtin_amdgcn_raw_buffer_load_b64(r, off, 0, 16); }
DEV void wsst(rsrc_t r, u32 off, u32x4 v) { __builtin_amdgcn_raw_buffer_store_b128(v, r, off, 0, 16); }
DEV void wsst2(rsrc_t r, u32 off, u32x2 v) { __builtin_amdgcn_raw_buffer_store_b64(v, r, off, 0, 16); }
DEV u32 umax3(u32 a, u32 b, u32 c) { return max(max(a, b), c); }
DEV u32 vmax4(u32 m, u32x4 v) { return umax3(umax3(m, v[0], v[1]), v[2], v[3]); }
DEV u32 clampw(u32 v) { return min(v, 0xFFFFFFFEu); }
DEV u32x4 clamp4(u32x4 v) { return u32x4{clampw(v[0]), clampw(v[1]), clampw(v[2]), clampw(v[3])}; }
DEV u32 pk(float a, float b) { return __builtin_bit_cast(u32, __builtin_convertvector(f32x2{a, b}, bf16x2)); }
DEV float lo2f(u32 v) { return __uint_as_float(v << 16); }
DEV float hi2f(u32 v) { return __uint_as_float(v & 0xFFFF0000u); }
DEV float rbf(float f) { return lo2f(pk(f, 0.f)); }
DEV bf16x8 asbf(u32x4 v) { return __builtin_bit_cast(bf16x8, v); }
DEV f32x4 mfma16(u32x4 a, u32x4 b, f32x4 c) { return __builtin_amdgcn_mfma_f32_16x16x32_bf16(asbf(a), asbf(b), c, 0, 0, 0); }
DEV f32x16 mfma32(u32x4 a, u32x4 b, f32x16 c) { return __builtin_amdgcn_mfma_f32_32x32x16_bf16(asbf(a), asbf(b), c, 0, 0, 0); }
DEV u32x4 lds128(u32 a) { return *(LAS u32x4*)a; }
DEV void lds128w(u32 a, u32x4 v) { *(LAS u32x4*)a = v; }
DEV u32x2 ldstr(u32 a) { return __builtin_bit_cast(u32x2, __builtin_amdgcn_ds_read_tr16_b64_v4bf16((LAS bf16x4*)a)); }
DEV u32x4 ldstr2(u32 a0, u32 a1) { u32x2 x = ldstr(a0), y = ldstr(a1); return u32x4{x[0], x[1], y[0], y[1]}; }
DEV float ex2(float x) { return __builtin_amdgcn_exp2f(x); }

DEV float xor32_sum(float v) { auto r = __builtin_amdgcn_permlane32_swap(__float_as_uint(v), __float_as_uint(v), false, false); return __uint_as_float(r[0]) + __uint_as_float(r[1]); }
DEV float xor32_max(float v) { auto r = __builtin_amdgcn_permlane32_swap(__float_as_uint(v), __float_as_uint(v), false, false); return fmaxf(__uint_as_float(r[0]), __uint_as_float(r[1])); }
DEV float xor16_sum(float v) { auto r = __builtin_amdgcn_permlane16_swap(__float_as_uint(v), __float_as_uint(v), false, false); return __uint_as_float(r[0]) + __uint_as_float(r[1]); }
DEV float xor16_max(float v) { auto r = __builtin_amdgcn_permlane16_swap(__float_as_uint(v), __float_as_uint(v), false, false); return fmaxf(__uint_as_float(r[0]), __uint_as_float(r[1])); }
template <int CTRL> DEV float dpp(float v) { return __uint_as_float(__builtin_amdgcn_update_dpp(0u, __float_as_uint(v), CTRL, 0xF, 0xF, true)); }
constexpr int ROR8 = 0x128, ROR4 = 0x124, ROR2 = 0x122, ROR1 = 0x121;

#define STAMP(k)

#define POLL(N, V, CHECK, SLEEP, LOADS)                                  \
  _Pragma("unroll") for (int i = 0; i < N; ++i) pin(V[i]);               \
  _Pragma("nounroll") for (u32 sp = 0; sp < SPIN_CAP; ++sp) {            \
    u32 bad = 0;                                                         \
    _Pragma("unroll") for (int i = 0; i < N; ++i) bad |= CHECK;          \
    if (__builtin_amdgcn_ballot_w64(bad != 0) == 0) break;               \
    if (SLEEP) __builtin_amdgcn_s_sleep(SLEEP);                          \
    _Pragma("unroll") for (int i = 0; i < N; ++i) LOADS;                 \
    waitv<0>();                                                          \
    _Pragma("unroll") for (int i = 0; i < N; ++i) pin(V[i]);             \
  }
DEV void bar() { waitl(); __builtin_amdgcn_s_barrier(); CFENCE(); }
DEV bool allok(u32 bad) { return __builtin_amdgcn_ballot_w64(bad != 0) == 0; }
DEV u32 swz(int r) { return ((r & 3) << 2) | ((r >> 2) & 3); }

DEV void qpart(u32 wl, u32x4 xf, int lane, u32 sb, u32 sq) {
  const int g = lane >> 4, n = lane & 15, qq = n >> 2, p = lane & 3;
  f32x4 acc[4];
#pragma unroll
  for (int t = 0; t < 4; ++t) {
    const u32 a = wl + (8 * g + qq) * 128 + t * 32 + p * 8;
    acc[t] = mfma16(ldstr2(a, a + 512), xf, f32x4{0.f, 0.f, 0.f, 0.f});
  }
  CFENCE();
#pragma unroll
  for (int t = 0; t < 2; ++t) {
    const f32x4 u = acc[t], v = acc[2 + t];
    const bool lo = n < 8;
    *(LAS f32x4*)(wl + (n & 7) * sb + ((n >> 3) * 8 + t * 4 + g) * sq) = f32x4{lo ? u[0] : v[0], lo ? u[1] : v[1], lo ? u[2] : v[2], lo ? u[3] : v[3]};
  }
}
DEV u32x4 qsum(u32 qr, u32 tl, u32 th) {
  f32x4 q[8];
#pragma unroll
  for (int i = 0; i < 8; ++i) q[i] = *(LAS f32x4*)(qr + i * 4096);
  const f32x4 r = ((q[0] + q[1]) + (q[2] + q[3])) + ((q[4] + q[5]) + (q[6] + q[7]));
  return u32x4{pk(r[0], r[1]), pk(r[2], r[3]), tl, th};
}

__global__ __launch_bounds__(512, 2) void gm_kernel(Params P) {
  extern __shared__ __attribute__((aligned(16))) char smem[];
  const int tid = threadIdx.x, lane = tid & 63;
  const int w = __builtin_amdgcn_readfirstlane(tid >> 6);
  const int wg = blockIdx.x, b = wg & 7, idx = (wg >> 3) & 15;
  const int h = HMAP, jq = idx >> 2;
  const rsrc_t ws = __builtin_amdgcn_make_buffer_rsrc(P.ws, 0, WS_BYTES, 0x00020000);
  const int n = lane & 15, g = lane >> 4, qq = n >> 2, p = lane & 3;
  STAMP(0);
  (void)smem;
  const char* pc; u32 cl0, cl1, pf = 0;
  asm volatile("s_getpc_b64 %0\n.Lgm_pc:\n\ts_mov_b32 %1, .Lgm_end-.Lgm_pc\n\ts_mov_b32 %2, .Lgm_end2-.Lgm_pc" : "=s"(pc), "=s"(cl0), "=s"(cl1));
  const u32 clen = max(cl0, cl1);
#define CODE_PF asm volatile("global_load_dword %0, %1, %2" : "=v"(pf) : "v"(min((u32)tid * 64u, clen)), "s"(pc) : "memory")
#define CODE_PIN asm volatile("" : "+v"(pf))
  if (wg < 128) {
    const int hkv = idx >> 1, half = idx & 1;
    u32x4 ep, xf;
    if (PFM == 2) CODE_PF;
    ldg_sc1(ep, 0u, P.ws);
    const size_t qoff = (size_t)((jq >> 1) * 256 + w * 32) * 8192 + h * 256 + (jq & 1) * 128;
#pragma unroll
    for (int s = 0; s < 4; ++s) dma_nt((u32)((lane >> 3) * 8192 + (lane & 7) * 16), P.wqa + qoff + s * 65536, L_WQA + w * 4096 + s * 1024);
    ldg(xf, (u32)((lane & 7) * 1024 + g * 16), P.x + (jq >> 1) * 512 + w * 64);
    bar();
    if (w < 2) {
#pragma unroll
      for (int k = 0; k < 16; ++k)
        dma_nt((u32)(g * 8192 + ((n ^ k) * 16)), P.wuk + (size_t)(jq * 128 + w * 64 + k * 4) * 8192 + h * 256, L_WUK + w * 16384 + k * 1024);
      waitv<16>();
    } else {
      if (w == 4) {
#pragma unroll
        for (int k = 0; k < 2; ++k) wsst(ws, WS_QL + ((4 * k + g) * 32 + h) * 1024 + jq * 256 + n * 16, u32x4{SENT, SENT, SENT, SENT});
      }
      waitv<0>();
    }
    pin(ep); pin(xf); CODE_PIN;
    const u64 epoch = (((u64)__builtin_amdgcn_readfirstlane(ep[1]) << 32) | (u32)__builtin_amdgcn_readfirstlane(ep[0])) + 0x0101010101010101ull;
    const u32 tl = (u32)epoch, th = (u32)(epoch >> 32);
    STAMP(1);
    if (tid == 256) wsst(ws, WS_FQ + (h * 4 + jq) * 16, u32x4{0, 0, tl, th});
    qpart(L_WQA + w * 4096, xf, lane, 16, 128);
    bar();
    if ((w >> 1) == 1) wsst(ws, WS_QA + h * 8192 + (2 * (jq & 1) + (w & 1)) * 2048 + (n >> 3) * 1024 + g * 256 + (jq >> 1) * 128 + (lane & 7) * 16, qsum(L_WQA + (w & 1) * 1024 + lane * 16, tl, th));
    STAMP(2);
    const size_t kvoff = ((size_t)(b * 8 + hkv) * 4096 + 3072 + half * 512) * 256;
#define KMAP(j) (w * 4 + (((j) + w + idx + b) & 3))
#define K_ISSUE(R0, CNT, WIN)                                                          \
  _Pragma("nounroll") for (int r = R0; r < R0 + CNT; ++r) {                            \
    waitv<WIN - 1>();                                                                  \
    dma_nt((u32)((g * 256 + (n ^ g) * 16) ^ ((r & 3) * 64)), P.kc + kvoff + KMAP(r >> 2) * 4096 + (r & 3) * 1024, w * 16384 + r * 1024); \
  }
    if (w < 2) {
      u32x4 uk[16], v[8];
      waitv<0>();
      STAMP(10);
#pragma unroll
      for (int t = 0; t < 4; ++t)
#pragma unroll
        for (int s = 0; s < 4; ++s) uk[t * 4 + s] = lds128(L_WUK + w * 16384 + (4 * n + t) * 256 + (((4 * s + g) ^ n) * 16));
      v[0][2] = ~tl;
      POLL(8, v, (v[i][2] ^ tl) | (v[i][3] ^ th), 0, ldg_sc1(v[i], (u32)(lane * 16), P.ws + WS_QA + h * 8192 + i * 1024))
      STAMP(3);
      f32x4 d[4];
#pragma unroll
      for (int t = 0; t < 4; ++t) {
        d[t] = f32x4{0.f, 0.f, 0.f, 0.f};
#pragma unroll
        for (int s = 0; s < 4; ++s) d[t] = mfma16(u32x4{v[2 * s][0], v[2 * s][1], v[2 * s + 1][0], v[2 * s + 1][1]}, uk[t * 4 + s], d[t]);
      }
#pragma unroll
      for (int r = 0; r < 2; ++r) {
        float e[4];
#pragma unroll
        for (int t = 0; t < 4; ++t) {
          auto s2 = __builtin_amdgcn_permlane32_swap(__float_as_uint(d[t][r]), __float_as_uint(d[t][2 + r]), false, false);
          e[t] = (__uint_as_float(s2[0]) + __uint_as_float(s2[1])) * SCL;
        }
        const int bb = 4 * (g & 1) + 2 * (g >> 1) + r;
        wsst2(ws, WS_QL + (bb * 32 + h) * 1024 + jq * 256 + w * 128 + n * 8, u32x2{clampw(pk(e[0], e[1])), clampw(pk(e[2], e[3]))});
      }
    }
    bar();
    STAMP(4);
#pragma unroll
    for (int s = 0; s < 4; ++s) dma_nt((u32)((lane >> 3) * 8192 + (lane & 7) * 16), P.wqb + qoff + s * 65536, L_WQB + w * 4096 + s * 1024);
#pragma unroll
    for (int k = 0; k < 4; ++k) wsst(ws, WS_AP + (h * 4 + jq) * 32768 + (w * 4 + k) * 1024 + lane * 16, u32x4{SENT, SENT, SENT, SENT});
    u32x4 vv[16], pf[2];
    float mx, l;
    f32x4 o[8];
#define V_LOAD ldg_nt(vv[i], (u32)(g * 1024 + n * 16), P.vc + kvoff + KMAP(i >> 2) * 4096 + (i & 3) * 256)
#define V_ISSUE(I0, I1)                                                                                                       \
  _Pragma("unroll") for (int i = I0; i < I1; ++i) {                                                                           \
    waitv<WB - 1>();                                                                                                          \
    V_LOAD;                                                                                                                   \
  }
#define PV_STEP(gm)                                                                                   \
  _Pragma("unroll") for (int k = 0; k < 8; ++k) pin(vv[gm * 8 + k]);                                  \
  _Pragma("unroll") for (int c = 0; c < 4; ++c) {                                                     \
    u32x4 t0, t1;                                                                                     \
    _Pragma("unroll") for (int jp = 0; jp < 4; ++jp) {                                                \
      const u32 a = vv[gm * 8 + 2 * jp][c], bq = vv[gm * 8 + 2 * jp + 1][c];                          \
      t0[jp] = __builtin_amdgcn_perm(bq, a, 0x05040100);                                              \
      t1[jp] = __builtin_amdgcn_perm(bq, a, 0x07060302);                                              \
    }                                                                                                 \
    o[2 * c] = mfma16(pf[gm], t0, gm ? o[2 * c] : f32x4{0.f, 0.f, 0.f, 0.f});                         \
    o[2 * c + 1] = mfma16(pf[gm], t1, gm ? o[2 * c + 1] : f32x4{0.f, 0.f, 0.f, 0.f});                 \
  }
    waitv<0>();
    qpart(L_WQB + w * 4096, xf, lane, 256, 16);
    bar();
    if ((w >> 1) == 1) wsst(ws, WS_QB + ((((w & 1) * 4 + g) * 2 + (jq >> 1)) * 32 + h) * 512 + (jq & 1) * 256 + n * 16, qsum(L_WQB + (w & 1) * 1024 + lane * 16, tl, th));
    if (w == 4 && lane == 0) wsst(ws, WS_FP + (jq * 32 + h) * 16, u32x4{0, 0, tl, th});
    STAMP(5);
    __builtin_amdgcn_s_sleep(BDLY);
    K_ISSUE(0, 16, WB)
    if (w == 0) {
      u32x4 v[4];
      v[0][2] = ~tl;
      POLL(4, v, (v[i][2] ^ tl) | (v[i][3] ^ th), 0, ldg_sc1(v[i], (u32)(lane * 16), P.ws + WS_QB + ((b * 2 + (i >> 1)) * 32 + hkv * 4) * 512 + (i & 1) * 1024))
#pragma unroll
      for (int i = 0; i < 2; ++i)
        *(LAS u32x2*)(L_QBS + i * 512 + lane * 8) = u32x2{pk(lo2f(v[i][0]) + lo2f(v[2 + i][0]), hi2f(v[i][0]) + hi2f(v[2 + i][0])), pk(lo2f(v[i][1]) + lo2f(v[2 + i][1]), hi2f(v[i][1]) + hi2f(v[2 + i][1]))};
    }
    V_ISSUE(0, WB)
      {
        waitv<WB>();
#pragma unroll
        for (int i = WB; i < WB + VX; ++i) V_LOAD;
        STAMP(6);
        bar();
        u32x4 qb[4];
#pragma unroll
        for (int s = 0; s < 4; ++s) qb[s] = lds128(L_QBS + (n & 3) * 256 + s * 64 + g * 16);
        f32x4 sc[4];
        mx = -3.0e38f;
#pragma unroll
        for (int t = 0; t < 4; ++t) {
          sc[t] = f32x4{0.f, 0.f, 0.f, 0.f};
#pragma unroll
          for (int s = 0; s < 4; ++s) sc[t] = mfma16(lds128(w * 16384 + (t * 16 + n) * 256 + (((4 * s + g) ^ n) * 16)), qb[s], sc[t]);
          sc[t] *= SCL;
          mx = fmaxf(fmaxf(mx, fmaxf(sc[t][0], sc[t][1])), fmaxf(sc[t][2], sc[t][3]));
        }
        mx = xor32_max(xor16_max(mx));
        l = 0.f;
#pragma unroll
        for (int t = 0; t < 4; ++t)
#pragma unroll
          for (int e = 0; e < 4; ++e) { sc[t][e] = ex2(sc[t][e] - mx); l += sc[t][e]; }
        l = xor32_sum(xor16_sum(l));
#pragma unroll
        for (int gm = 0; gm < 2; ++gm)
          pf[gm] = u32x4{pk(sc[2 * gm][0], sc[2 * gm][1]), pk(sc[2 * gm][2], sc[2 * gm][3]), pk(sc[2 * gm + 1][0], sc[2 * gm + 1][1]), pk(sc[2 * gm + 1][2], sc[2 * gm + 1][3])};
        if (lane < 4) *(LAS f32x2*)(L_BML + lane * 64 + w * 8) = f32x2{mx, l};
        STAMP(7);
      }
    V_ISSUE(WB + VX, 12)
    bar();
    const int hh = 2 * (w & 1) + (lane >> 5), un = lane & 31;
    float me[8], mlo;
    {
      f32x4 ml[4];
#pragma unroll
      for (int i = 0; i < 4; ++i) ml[i] = *(LAS f32x4*)(L_BML + hh * 64 + i * 16);
      float M = ml[0][0];
#pragma unroll
      for (int i = 1; i < 8; ++i) M = fmaxf(M, ml[i >> 1][(i & 1) * 2]);
      float L = 0.f;
#pragma unroll
      for (int i = 0; i < 8; ++i) {
        me[i] = ex2(ml[i >> 1][(i & 1) * 2] - M);
        L += me[i] * ml[i >> 1][(i & 1) * 2 + 1];
      }
      mlo = (un & 1) ? L : M;
    }
    V_ISSUE(12, 16)
    waitv<8>();
    PV_STEP(0)
    waitv<0>();
    STAMP(8);
    PV_STEP(1)
    if (lane < 16) {
#pragma unroll
      for (int k = 0; k < 4; ++k) {
        *(LAS f32x4*)(L_BM + w * 2048 + k * 512 + n * 32) = f32x4{o[0][k], o[1][k], o[2][k], o[3][k]};
        *(LAS f32x4*)(L_BM + w * 2048 + k * 512 + n * 32 + 16) = f32x4{o[4][k], o[5][k], o[6][k], o[7][k]};
      }
    }
    bar();
    if (w < 2) {
      f32x4 r = {0.f, 0.f, 0.f, 0.f}, r1 = r;
#pragma unroll
      for (int i = 0; i < 4; ++i) {
        r += me[i] * *(LAS f32x4*)(L_BM + i * 2048 + hh * 512 + un * 16);
        r1 += me[4 + i] * *(LAS f32x4*)(L_BM + (4 + i) * 2048 + hh * 512 + un * 16);
      }
      r += r1;
      wsst(ws, WS_BP + (((hkv * 4 + hh) * 2 + half) * 8 + b) * 512 + un * 16, u32x4{pk(r[0], r[1]), pk(r[2], r[3]), tl, __float_as_uint(mlo)});
    }
    asm volatile(".Lgm_end2:");
    STAMP(9);
  } else {
    const char* cb = P.ckv + ((size_t)b * 4096 + idx * 256 + w) * 1024;
#define AROT (2 * b)
#define CROW(i) (cb + (((i) & 16) | (((i) + (AROT)) & 15)) * 8192)
    u32x4 ep, sg[16], v[4], fl[1];
    if (PFM) CODE_PF;
    ldg_sc1(ep, 0u, P.ws);
#define CKV_ISSUE(I0, I1)                                                                                   \
  _Pragma("unroll") for (int i = I0; i < I1; ++i) {                                                         \
    if (i >= WA) waitv<WA - 1>();                                                                           \
    dma_nt((u32)((lane ^ swz(i * 8 + w)) * 16), CROW(i), (u32)((i * 8 + w) * 1024));        \
  }
    __builtin_amdgcn_s_sleep(ADLY);
    CKV_ISSUE(0, WA + 1)
    pin(ep); CODE_PIN;
    const u64 epoch = (((u64)__builtin_amdgcn_readfirstlane(ep[1]) << 32) | (u32)__builtin_amdgcn_readfirstlane(ep[0])) + 0x0101010101010101ull;
    const u32 tl = (u32)epoch, th = (u32)(epoch >> 32);
    CKV_ISSUE(WA + 1, 16)
#define FQ_LOAD ldg_sc1(fl[0], (u32)(n * 16), P.ws + WS_FQ + w * 256)
    FQ_LOAD;
    bar();
    STAMP(1);
#define QL_LOAD ldg_sc1(v[i], (u32)(lane * 16), P.ws + WS_QL + (b * 32 + 4 * w + i) * 1024)
#define SG_LOAD ldg_nt(sg[i], (u32)((lane ^ swz(i * 8 + w)) * 16), CROW(16 + i))
#pragma unroll
    for (int i = 0; i < NPRE; ++i) { waitv<WA - 1>(); SG_LOAD; }
    waitv<NPRE>();
    POLL(1, fl, (fl[i][2] ^ tl) | (fl[i][3] ^ th), 1, FQ_LOAD)
    STAMP(11);
    if (PFM == 3) CODE_PF;
#pragma unroll
    for (int i = 0; i < 4; ++i) QL_LOAD;
#pragma unroll
    for (int i = NPRE; i < NPRE + NMID; ++i) { waitv<WA + 3>(); SG_LOAD; }
    waitv<NMID>();
    POLL(4, v, (u32)(vmax4(0, v[i]) == SENT), 1, rel_sc1(v[i], vmax4(0, v[i]) == SENT, (u32)(lane * 16), P.ws + WS_QL + (b * 32 + 4 * w + i) * 1024))
    CODE_PIN;
    STAMP(2);
#pragma unroll
    for (int i = NPRE + NMID; i < NPRE + NMID + NSTEP; ++i) SG_LOAD;
#pragma unroll
    for (int i = 0; i < 4; ++i) lds128w(L_X + (4 * w + i) * 1024 + ((lane ^ swz(4 * w + i)) * 16), v[i]);
    bar();
    const int r32 = lane & 31, hf = lane >> 5, kg = w & 3, hh2 = w >> 2;
    const u32 fb = n * 1024 + 16 * ((4 * g) ^ swz(n));
    u32x4 qf[16];
#pragma unroll
    for (int ks = 0; ks < 16; ++ks) qf[ks] = lds128(L_X + hh2 * 16384 + (fb ^ (16 * (ks & 3))) + 256 * (ks >> 2));
    f32x16 o[2];
    float m_run = -3.0e38f, m_sc = -3.0e38f, l_run = 0.f;
#pragma unroll
    for (int ph = 0; ph < 2; ++ph) {
      f32x4 sc[2] = {{0.f, 0.f, 0.f, 0.f}, {0.f, 0.f, 0.f, 0.f}};
#pragma unroll
      for (int ks = 0; ks < 16; ++ks)
#pragma unroll
        for (int t = 0; t < 2; ++t) sc[t] = mfma16(lds128(kg * 32768 + t * 16384 + (fb ^ (16 * (ks & 3))) + 256 * (ks >> 2)), qf[ks], sc[t]);
#define AP_OFF(k) (WS_AP + ((((8 * k + (lane >> 3)) * 4 + (w >> 1)) * 8 + b) * 16 + idx) * 256 + (w & 1) * 128 + (lane & 7) * 16)
      if (ph == 0) {
#pragma unroll
        for (int i = NPRE + NMID + NSTEP; i < NPRE + NMID + 2 * NSTEP && i < 16; ++i) SG_LOAD;
      }
      if (ph == 1 && WOAP == 0) {
#pragma unroll
        for (int i = 4; i < 8; ++i) ldg_nt(sg[i], (u32)(g * 1024 + n * 16), P.woa + (size_t)(h * 128 + (4 * w + (i & 3)) * 4) * 1024 + jq * 256);
      }
      float mx = fmaxf(fmaxf(fmaxf(sc[0][0], sc[0][1]), fmaxf(sc[0][2], sc[0][3])), fmaxf(fmaxf(sc[1][0], sc[1][1]), fmaxf(sc[1][2], sc[1][3])));
      mx = xor32_max(xor16_max(mx));
      if (ph == 0) bar();
      if (lane < 16) *(LAS float*)(L_MAXB + (hh2 * 16 + n) * 16 + kg * 4) = mx;
      bar();
      const f32x4 ma = *(LAS f32x4*)(L_MAXB + (hh2 * 16 + n) * 16), mb = *(LAS f32x4*)(L_MAXB + r32 * 16);
      mx = fmaxf(fmaxf(fmaxf(ma[0], ma[1]), fmaxf(ma[2], ma[3])), m_sc);
      float l = 0.f;
#pragma unroll
      for (int t = 0; t < 2; ++t) {
#pragma unroll
        for (int i = 0; i < 4; ++i) { sc[t][i] = ex2(sc[t][i] - mx); l += sc[t][i]; }
        *(LAS u32x2*)(L_P + (2 * kg + t) * 1024 + ((g & 1) * 32 + hh2 * 16 + n) * 16 + (g >> 1) * 8) = u32x2{pk(sc[t][0], sc[t][1]), pk(sc[t][2], sc[t][3])};
      }
      l_run = l_run * ex2(m_sc - mx) + l;
      m_sc = mx;
      if (ph == 1) {
        const float ls = xor32_sum(xor16_sum(l_run));
        if (lane < 16) *(LAS float*)(L_SUMB + (hh2 * 16 + n) * 16 + kg * 4) = ls;
      }
      const float mo = fmaxf(fmaxf(fmaxf(mb[0], mb[1]), fmaxf(mb[2], mb[3])), m_run);
      const float alpha = ex2(m_run - mo);
      m_run = mo;
#pragma unroll
      for (int ct = 0; ct < 2; ++ct)
#pragma unroll
        for (int i = 0; i < 16; ++i) o[ct][i] = ph ? o[ct][i] * alpha : 0.f;
      bar();
      STAMP(3 + 3 * ph);
      if (ph == 0) {
#pragma unroll
        for (int i = NPRE + NMID + 2 * NSTEP; i < NPRE + NMID + 3 * NSTEP && i < 16; ++i) SG_LOAD;
      }
      if (ph == 1 && WOAP == 1) {
#pragma unroll
        for (int i = 4; i < 8; ++i) ldg_nt(sg[i], (u32)(g * 1024 + n * 16), P.woa + (size_t)(h * 128 + (4 * w + (i & 3)) * 4) * 1024 + jq * 256);
      }
      if (ph == 1 && w == 0) {
        const f32x4 a = *(LAS f32x4*)(L_SUMB + r32 * 16);
        const float lsum = (a[0] + a[1]) + (a[2] + a[3]);
        if (lane < 32) wsst(ws, WS_ML + ((b * 16 + idx) * 32 + lane) * 16, u32x4{__float_as_uint(m_run), __float_as_uint(lsum), tl, th});
      }
      {
        const int blk = g & 1;
        const u32 a0 = ((4 * hf + qq) * 1024 + (w >> 1) * 256 + 16 * ((2 * blk + (p >> 1)) ^ ((qq << 2) | hf)) + 8 * (p & 1)) ^ (128 * (w & 1));
        const u32 a1 = (a0 ^ 32) + 8192;
#pragma unroll
        for (int ks = 0; ks < 8; ++ks) {
          if (ph == 0 && ks == 4) {
#pragma unroll
            for (int i = NPRE + NMID + 3 * NSTEP; i < 16; ++i) SG_LOAD;
          }
          const u32x4 pf = lds128(L_P + ks * 1024 + lane * 16);
#pragma unroll
          for (int ct = 0; ct < 2; ++ct) o[ct] = mfma32(ldstr2((a0 ^ (64 * ct)) + 16384 * ks, (a1 ^ (64 * ct)) + 16384 * ks), pf, o[ct]);
        }
      }
      if (ph == 0) {
        bar();
        STAMP(4);
        waitv<0>();
#pragma unroll
        for (int i = 0; i < 16; ++i) { pin(sg[i]); lds128w((i * 8 + w) * 1024 + lane * 16, sg[i]); }
#define FP_LOAD ldg_sc1(fl[0], (u32)(r32 * 16), P.ws + WS_FP + (w >> 1) * 512)
        waitl();
        FP_LOAD;
        ldg(sg[8], (u32)(lane * 16), P.gw);
        ldg(sg[9], (u32)(lane * 16), P.x + w * 1024);
#pragma unroll
        for (int i = 0; i < 4; ++i) ldg_nt(sg[i], (u32)(g * 8192 + n * 16), P.wuv + (size_t)(jq * 128 + (4 * w + i) * 4) * 8192 + h * 256);
        bar();
        STAMP(5);
      }
    }
    STAMP(7);
#define AP_ROW(r) ((r) * 1024 + (w >> 1) * 256 + (((w ^ ((r) >> 1)) & 1) * 128))
    CFENCE();
#pragma unroll
    for (int ct = 0; ct < 2; ++ct)
#pragma unroll
      for (int uu = 0; uu < 2; ++uu)
        lds128w(AP_ROW(r32) + (((ct * 4 + hf * 2 + uu) ^ (r32 & 7)) * 16),
                clamp4(u32x4{pk(o[ct][8 * uu], o[ct][8 * uu + 1]), pk(o[ct][8 * uu + 2], o[ct][8 * uu + 3]), pk(o[ct][8 * uu + 4], o[ct][8 * uu + 5]), pk(o[ct][8 * uu + 6], o[ct][8 * uu + 7])}));
    waitv<8>();
    POLL(1, fl, (fl[i][2] ^ tl) | (fl[i][3] ^ th), 1, FP_LOAD)
    waitl();
#pragma unroll
    for (int k = 0; k < 4; ++k) {
      const int hh = 8 * k + (lane >> 3);
      wsst(ws, AP_OFF(k), lds128(AP_ROW(hh) + (((lane & 7) ^ (hh & 7)) * 16)));
    }
    STAMP(8);
    u32x4 wu[4], wa[4], wb[4];
    float gate;
    {
      u32x4 mlv[1];
      ldg_sc1(mlv[0], (u32)(n * 512), P.ws + WS_ML + (w * 512 + h) * 16);
      bar();
      waitv<5>();
#pragma unroll
      for (int i = 0; i < 8; ++i) { pin(sg[i]); lds128w((i >> 2) * 32768 + (4 * w + (i & 3)) * 1024 + lane * 16, sg[i]); }
      bar();
#pragma unroll
      for (int i = 0; i < 4; ++i) {
        const u32 a = L_WUV + (32 * i + 16 * (g & 1) + 4 * (g >> 1) + qq) * 256 + w * 32 + p * 8;
        wu[i] = ldstr2(a, a + 2048);
        const u32 a2 = L_WOA + (32 * i + 8 * g + qq) * 256 + w * 32 + p * 8;
        wa[i] = ldstr2(a2, a2 + 1024);

      }
      pin(sg[8]); pin(sg[9]);
    {
      float a = 0.f;
#pragma unroll
      for (int e = 0; e < 4; ++e) a += rbf(lo2f(sg[9][e]) * lo2f(sg[8][e])) + rbf(hi2f(sg[9][e]) * hi2f(sg[8][e]));
      a += dpp<ROR1>(a); a += dpp<ROR2>(a); a += dpp<ROR4>(a); a += dpp<ROR8>(a);
      a = xor32_sum(xor16_sum(a));
      gate = rbf(1.f / (1.f + __expf(-rbf(a))));
    }
      if (lane == 0) *(LAS float*)(L_CL + 4096 + w * 4) = gate;
      waitv<0>();
      POLL(1, mlv, (mlv[i][2] ^ tl) | (mlv[i][3] ^ th), 2, ldg_sc1(mlv[i], (u32)(n * 512), P.ws + WS_ML + (w * 512 + h) * 16))
      const u32x4 ml = mlv[0];
#pragma unroll
      for (int i = 0; i < 4; ++i) ldg_sc1(v[i], (u32)(lane * 16), P.ws + WS_AP + ((h * 4 + jq) * 8 + w) * 4096 + i * 1024);
      const float ms = __uint_as_float(ml[0]);
      float M = ms;
      M = fmaxf(M, dpp<ROR1>(M)); M = fmaxf(M, dpp<ROR2>(M)); M = fmaxf(M, dpp<ROR4>(M)); M = fmaxf(M, dpp<ROR8>(M));
      const float e = ex2(ms - M);
      float L = e * __uint_as_float(ml[1]);
      L += dpp<ROR1>(L); L += dpp<ROR2>(L); L += dpp<ROR4>(L); L += dpp<ROR8>(L);
      const float wgt = e * __builtin_amdgcn_rcpf(L);
      float wv[4], acc[8];
#pragma unroll
      for (int i = 0; i < 4; ++i) wv[i] = __uint_as_float(__builtin_amdgcn_ds_bpermute((4 * i + g) * 4, __float_as_uint(wgt)));
      waitv<0>();
      POLL(4, v, (u32)(vmax4(0, v[i]) == SENT), 1, rel_sc1(v[i], vmax4(0, v[i]) == SENT, (u32)(lane * 16), P.ws + WS_AP + ((h * 4 + jq) * 8 + w) * 4096 + i * 1024))
      STAMP(9);
#pragma unroll
      for (int i = 0; i < 4; ++i) dma_nt((u32)(g * 1024 + n * 16), P.wob + (size_t)(h * 128 + (4 * w + i) * 4) * 1024 + jq * 256, L_WOB + (4 * w + i) * 1024);
#pragma unroll
      for (int i = 0; i < 4; ++i) {
#pragma unroll
        for (int e2 = 0; e2 < 4; ++e2) {
          acc[2 * e2] = (i ? acc[2 * e2] : 0.f) + wv[i] * lo2f(v[i][e2]);
          acc[2 * e2 + 1] = (i ? acc[2 * e2 + 1] : 0.f) + wv[i] * hi2f(v[i][e2]);
        }
      }
      float r4[4], r2[2];
#pragma unroll
      for (int e2 = 0; e2 < 4; ++e2) {
        auto r = __builtin_amdgcn_permlane32_swap(__float_as_uint(acc[e2]), __float_as_uint(acc[4 + e2]), false, false);
        r4[e2] = __uint_as_float(r[0]) + __uint_as_float(r[1]);
      }
#pragma unroll
      for (int e2 = 0; e2 < 2; ++e2) {
        auto r = __builtin_amdgcn_permlane16_swap(__float_as_uint(r4[e2]), __float_as_uint(r4[2 + e2]), false, false);
        r2[e2] = __uint_as_float(r[0]) + __uint_as_float(r[1]);
      }
      *(LAS u32*)(L_CL + (n * 8 + w) * 16 + g * 4) = pk(r2[0], r2[1]);
    }
    bar();
    STAMP(10);
    {
      f32x4 d = {0.f, 0.f, 0.f, 0.f};
#pragma unroll
      for (int ks = 0; ks < 4; ++ks) {
        d = mfma16(wu[ks], lds128(L_CL + ((ks * 4 + g) * 8 + (n & 7)) * 16), d);
      }
      if (n < 8) wsst(ws, WS_CT + (h * 4 + jq) * 4096 + (w * 4 + g) * 128 + n * 16, u32x4{pk(d[0], d[1]), pk(d[2], d[3]), tl, th});
    }
    {
      const f32x4 gg = *(LAS f32x4*)(L_CL + 4096 + (g & 1) * 16);
#define CB_ADDR (u32)(hf * 4096 + r32 * 16), P.ws + (u32)__builtin_amdgcn_readfirstlane((i < 2 ? WS_CT + (h * 4 + 2 * i) * 4096 : WS_BP + h * 8192) + w * 512)
#define CB_BAD ((v[i][2] ^ tl) | (i < 2 ? v[i][3] ^ th : 0u))
      waitv<0>();
#pragma unroll
      for (int i = 0; i < 3; ++i) ldg_sc1(v[i], CB_ADDR);
      bar();
#pragma unroll
      for (int i = 0; i < 4; ++i) {
        const u32 a = L_WOB + (32 * i + 8 * g + qq) * 256 + w * 32 + p * 8;
        wb[i] = ldstr2(a, a + 1024);
      }
      waitv<0>();
      POLL(3, v, CB_BAD, 0, rel_sc1(v[i], CB_BAD != 0, CB_ADDR))
      STAMP(12);
      {
        auto s0 = __builtin_amdgcn_permlane32_swap(__float_as_uint(lo2f(v[0][0]) + lo2f(v[1][0])), __float_as_uint(lo2f(v[0][1]) + lo2f(v[1][1])), false, false);
        auto s1 = __builtin_amdgcn_permlane32_swap(__float_as_uint(hi2f(v[0][0]) + hi2f(v[1][0])), __float_as_uint(hi2f(v[0][1]) + hi2f(v[1][1])), false, false);
        *(LAS u32*)(L_CL + 8192 + ((2 * w + (r32 >> 4)) * 8 + (r32 & 7)) * 16 + ((r32 >> 3) & 1) * 8 + hf * 4) =
            pk(__uint_as_float(s0[0]) + __uint_as_float(s0[1]), __uint_as_float(s1[0]) + __uint_as_float(s1[1]));
      }
      {
        const float x = __uint_as_float(v[2][3]), y = dpp<0x141>(x);
        const float m0 = (lane & 1) ? y : x, l0 = (lane & 1) ? x : y;
        const float e = ex2(m0 - xor32_max(m0));
        const float wgt = e * rbf(1.f - gate) * __builtin_amdgcn_rcpf(xor32_sum(e * l0));
        auto s0 = __builtin_amdgcn_permlane32_swap(__float_as_uint(wgt * lo2f(v[2][0])), __float_as_uint(wgt * lo2f(v[2][1])), false, false);
        auto s1 = __builtin_amdgcn_permlane32_swap(__float_as_uint(wgt * hi2f(v[2][0])), __float_as_uint(wgt * hi2f(v[2][1])), false, false);
        *(LAS u32*)(L_CL + 12288 + ((r32 >> 1) * 8 + w) * 16 + (r32 & 1) * 8 + hf * 4) =
            pk(__uint_as_float(s0[0]) + __uint_as_float(s0[1]), __uint_as_float(s1[0]) + __uint_as_float(s1[1]));
      }
      bar();
      STAMP(13);
      f32x4 da = {0.f, 0.f, 0.f, 0.f}, db = {0.f, 0.f, 0.f, 0.f};
#pragma unroll
      for (int i = 0; i < 4; ++i) {
        da = mfma16(lds128(L_CL + 8192 + ((4 * i + g) * 8 + (n & 7)) * 16), wa[i], da);
        db = mfma16(lds128(L_CL + 12288 + ((4 * i + g) * 8 + (n & 7)) * 16), wb[i], db);
      }
      da = da * gg + db;
      u32x4 ov;
#pragma unroll
      for (int r = 0; r < 4; ++r) ov[r] = (__float_as_uint(da[r]) & 0xFFFFFF00u) | ((tl >> (8 * r)) & 0xFFu);
      if (lane < 32) wsst(ws, WS_OP + ((jq * 32 + w * 4 + (lane >> 3)) * 32 + h) * 128 + (lane & 7) * 16, ov);
      STAMP(14);
      if (w == 0) {
        const int red = wg - 128;
        waitv<0>();
        v[0][0] = ~tl;
        POLL(4, v, __builtin_amdgcn_perm(__builtin_amdgcn_perm(v[i][3], v[i][2], 0x0c0c0400), __builtin_amdgcn_perm(v[i][1], v[i][0], 0x0c0c0400), 0x05040100) ^ tl, 0,
             ldg_sc1(v[i], (u32)(lane * 16), P.ws + WS_OP + red * 4096 + i * 1024))
        float r[4];
#pragma unroll
        for (int e2 = 0; e2 < 4; ++e2) {
          float x = (__uint_as_float(v[0][e2] & 0xFFFFFF00u) + __uint_as_float(v[1][e2] & 0xFFFFFF00u)) + (__uint_as_float(v[2][e2] & 0xFFFFFF00u) + __uint_as_float(v[3][e2] & 0xFFFFFF00u));
          x += dpp<ROR8>(x);
          r[e2] = xor32_sum(xor16_sum(x));
        }
        const float rv = g == 0 ? r[0] : g == 1 ? r[1] : g == 2 ? r[2] : r[3];
        const int L = red & 7, col = (red >> 3) * 32 + 16 * (L >> 2) + 8 * (L & 1) + n, bb = 4 * ((L >> 1) & 1) + g;
        asm volatile(".Lgm_end:");
        if (n < 8) *(unsigned short*)(P.out + (bb * 512 + col) * 2) = (unsigned short)pk(rv, 0.f);
        if (wg == 128 && lane == 0) wsst2(ws, WS_EPOCH, u32x2{tl, th});
        STAMP(15);
      }
    }
  }
}

extern "C" void kernel_launch(void* const* d_in, const int* in_sizes, int n_in, void* d_out, int out_size, void* d_ws, size_t ws_size, hipStream_t stream) {
  Params p;
  p.x = (const char*)d_in[0]; p.ckv = (const char*)d_in[1]; p.wqa = (const char*)d_in[2]; p.wuk = (const char*)d_in[3];
  p.wuv = (const char*)d_in[4]; p.woa = (const char*)d_in[5]; p.kc = (const char*)d_in[7]; p.vc = (const char*)d_in[8];
  p.wqb = (const char*)d_in[9]; p.wob = (const char*)d_in[10]; p.gw = (const char*)d_in[11];
  p.out = (char*)d_out; p.ws = (char*)d_ws;
  (void)hipFuncSetAttribute((const void*)gm_kernel, hipFuncAttributeMaxDynamicSharedMemorySize, LDS_BYTES);
  hipLaunchKernelGGL(gm_kernel, dim3(256), dim3(512), LDS_BYTES, stream, p);
}
```

```cpp
#pragma clang diagnostic ignored "-Wint-to-pointer-cast"
#include <hip/hip_runtime.h>
#include <hip/hip_bf16.h>
#include <stdint.h>

typedef unsigned int u32;
typedef unsigned long long u64;
typedef u32 u32x4 __attribute__((ext_vector_type(4)));
typedef u32 u32x2 __attribute__((ext_vector_type(2)));
typedef float f32x4 __attribute__((ext_vector_type(4)));
typedef float f32x2 __attribute__((ext_vector_type(2)));
typedef float f32x16 __attribute__((ext_vector_type(16)));
typedef __bf16 bf16x8 __attribute__((ext_vector_type(8)));
typedef __bf16 bf16x4 __attribute__((ext_vector_type(4)));
typedef __bf16 bf16x2 __attribute__((ext_vector_type(2)));
#define LAS __attribute__((address_space(3)))
#define DEV __device__ __forceinline__

#define HMAP (b + 8 * (idx & 3))
#define WA 4
#define NPRE 8
#define NSTEP 4
#define NMID 0
#define WB 4
#define ADLY 16
#define BDLY 16
#define WOAP 0
#define VX 0
constexpr u32 WS_EPOCH = 0;
constexpr u32 WS_FQ   = 4096;
constexpr u32 WS_FP   = 8192;
constexpr u32 WS_QA   = 16384;
constexpr u32 WS_QB   = WS_QA + 262144;
constexpr u32 WS_QL   = WS_QB + 262144;
constexpr u32 WS_ML   = WS_QL + 262144;
constexpr u32 WS_CT   = WS_ML + 65536;
constexpr u32 WS_BP   = WS_CT + 524288;
constexpr u32 WS_OP   = WS_BP + 262144;
constexpr u32 WS_AP   = WS_OP + 524288;
constexpr u32 WS_BYTES = WS_AP + 4194304;
constexpr u32 WS_DBG = 16u << 20;

constexpr u32 L_X = 131072;
constexpr u32 L_P = L_X, L_MAXB = L_X + 8192, L_SUMB = L_X + 8704;
constexpr u32 L_WUV = 0, L_WOA = 32768, L_WOB = 65536, L_CL = 98304;
constexpr u32 L_WUK = 0, L_WQA = 65536, L_WQB = L_X;
constexpr u32 L_BM = L_X + 16384, L_BML = L_X + 3072, L_QBS = L_X + 2048;
constexpr u32 LDS_BYTES = 163840;
constexpr u32 SENT = 0xFFFFFFFFu;
#define DMA_Q dma_nt
constexpr u32 SPIN_CAP = 1u << 20;
constexpr float SCL = 0.08838834764831845f * 1.4426950408889634f;

struct Params {
  const char *x, *ckv, *wqa, *wuk, *wuv, *woa, *kc, *vc, *wqb, *wob, *gw;
  char *out, *ws;
};

template <int N> DEV void waitv() { asm volatile("s_waitcnt vmcnt(%0)" ::"i"(N) : "memory"); }
DEV void waitl() { asm volatile("s_waitcnt lgkmcnt(0)" ::: "memory"); }
DEV void ldg_nt(u32x4& r, u32 voff, const char* sb) { asm volatile("global_load_dwordx4 %0, %1, %2 nt" : "=v"(r) : "v"(voff), "s"(sb) : "memory"); }
DEV void ldg(u32x4& r, u32 voff, const char* sb) { asm volatile("global_load_dwordx4 %0, %1, %2" : "=v"(r) : "v"(voff), "s"(sb) : "memory"); }
DEV void ldg_sc1(u32x4& r, u32 voff, const char* sb) { asm volatile("global_load_dwordx4 %0, %1, %2 sc1" : "=v"(r) : "v"(voff), "s"(sb) : "memory"); }
DEV void rel_sc1(u32x4& r, bool miss, u32 voff, const char* sb) {
  asm volatile("s_mov_b64 exec, %3\n\tglobal_load_dwordx4 %0, %1, %2 sc1\n\ts_mov_b64 exec, -1" : "+v"(r) : "v"(voff), "s"(sb), "s"(__builtin_amdgcn_ballot_w64(miss)) : "memory");
}
DEV void dma_nt(u32 voff, const char* sb, u32 lds) {
  u32 keep;
  asm volatile("s_mov_b32 %0, m0\n\ts_mov_b32 m0, %3\n\ts_nop 0\n\tglobal_load_lds_dwordx4 %1, %2 nt\n\ts_mov_b32 m0, %0" : "=&s"(keep) : "v"(voff), "s"(sb), "s"(lds) : "memory");
}
DEV void dma(u32 voff, const char* sb, u32 lds) {
  u32 keep;
  asm volatile("s_mov_b32 %0, m0\n\ts_mov_b32 m0, %3\n\ts_nop 0\n\tglobal_load_lds_dwordx4 %1, %2\n\ts_mov_b32 m0, %0" : "=&s"(keep) : "v"(voff), "s"(sb), "s"(lds) : "memory");
}
DEV void pin(u32x4& r) { asm volatile("" : "+v"(r)); }
#define CFENCE() asm volatile("" ::: "memory")

typedef __amdgpu_buffer_rsrc_t rsrc_t;
DEV u32x4 wsld(rsrc_t r, u32 off) { return __builtin_amdgcn_raw_buffer_load_b128(r, off, 0, 16); }
DEV u32x2 wsld2(rsrc_t r, u32 off) { return __builtin_amdgcn_raw_buffer_load_b64(r, off, 0, 16); }
DEV void wsst(rsrc_t r, u32 off, u32x4 v) { __builtin_amdgcn_raw_buffer_store_b128(v, r, off, 0, 16); }
DEV void wsst2(rsrc_t r, u32 off, u32x2 v) { __builtin_amdgcn_raw_buffer_store_b64(v, r, off, 0, 16); }
DEV u32 umax3(u32 a, u32 b, u32 c) { return max(max(a, b), c); }
DEV u32 vmax4(u32 m, u32x4 v) { return umax3(umax3(m, v[0], v[1]), v[2], v[3]); }
DEV u32 clampw(u32 v) { return min(v, 0xFFFFFFFEu); }
DEV u32x4 clamp4(u32x4 v) { return u32x4{clampw(v[0]), clampw(v[1]), clampw(v[2]), clampw(v[3])}; }
DEV u32 pk(float a, float b) { return __builtin_bit_cast(u32, __builtin_convertvector(f32x2{a, b}, bf16x2)); }
DEV float lo2f(u32 v) { return __uint_as_float(v << 16); }
DEV float hi2f(u32 v) { return __uint_as_float(v & 0xFFFF0000u); }
DEV float rbf(float f) { return lo2f(pk(f, 0.f)); }
DEV bf16x8 asbf(u32x4 v) { return __builtin_bit_cast(bf16x8, v); }
DEV f32x4 mfma16(u32x4 a, u32x4 b, f32x4 c) { return __builtin_amdgcn_mfma_f32_16x16x32_bf16(asbf(a), asbf(b), c, 0, 0, 0); }
DEV f32x16 mfma32(u32x4 a, u32x4 b, f32x16 c) { return __builtin_amdgcn_mfma_f32_32x32x16_bf16(asbf(a), asbf(b), c, 0, 0, 0); }
DEV u32x4 lds128(u32 a) { return *(LAS u32x4*)a; }
DEV void lds128w(u32 a, u32x4 v) { *(LAS u32x4*)a = v; }
DEV u32x2 ldstr(u32 a) { return __builtin_bit_cast(u32x2, __builtin_amdgcn_ds_read_tr16_b64_v4bf16((LAS bf16x4*)a)); }
DEV u32x4 ldstr2(u32 a0, u32 a1) { u32x2 x = ldstr(a0), y = ldstr(a1); return u32x4{x[0], x[1], y[0], y[1]}; }
DEV float ex2(float x) { return __builtin_amdgcn_exp2f(x); }

DEV float xor32_sum(float v) { auto r = __builtin_amdgcn_permlane32_swap(__float_as_uint(v), __float_as_uint(v), false, false); return __uint_as_float(r[0]) + __uint_as_float(r[1]); }
DEV float xor32_max(float v) { auto r = __builtin_amdgcn_permlane32_swap(__float_as_uint(v), __float_as_uint(v), false, false); return fmaxf(__uint_as_float(r[0]), __uint_as_float(r[1])); }
DEV float xor16_sum(float v) { auto r = __builtin_amdgcn_permlane16_swap(__float_as_uint(v), __float_as_uint(v), false, false); return __uint_as_float(r[0]) + __uint_as_float(r[1]); }
DEV float xor16_max(float v) { auto r = __builtin_amdgcn_permlane16_swap(__float_as_uint(v), __float_as_uint(v), false, false); return fmaxf(__uint_as_float(r[0]), __uint_as_float(r[1])); }
template <int CTRL> DEV float dpp(float v) { return __uint_as_float(__builtin_amdgcn_update_dpp(0u, __float_as_uint(v), CTRL, 0xF, 0xF, true)); }
constexpr int ROR8 = 0x128, ROR4 = 0x124, ROR2 = 0x122, ROR1 = 0x121;

#define STAMP(k)

#define POLL(N, V, CHECK, SLEEP, LOADS)                                  \
  _Pragma("unroll") for (int i = 0; i < N; ++i) pin(V[i]);               \
  _Pragma("nounroll") for (u32 sp = 0; sp < SPIN_CAP; ++sp) {            \
    u32 bad = 0;                                                         \
    _Pragma("unroll") for (int i = 0; i < N; ++i) bad |= CHECK;          \
    if (__builtin_amdgcn_ballot_w64(bad != 0) == 0) break;               \
    if (SLEEP) __builtin_amdgcn_s_sleep(SLEEP);                          \
    _Pragma("unroll") for (int i = 0; i < N; ++i) LOADS;                 \
    waitv<0>();                                                          \
    _Pragma("unroll") for (int i = 0; i < N; ++i) pin(V[i]);             \
  }
DEV void bar() { waitl(); __builtin_amdgcn_s_barrier(); CFENCE(); }
DEV bool allok(u32 bad) { return __builtin_amdgcn_ballot_w64(bad != 0) == 0; }
DEV u32 swz(int r) { return ((r & 3) << 2) | ((r >> 2) & 3); }

DEV void qpart(u32 wl, u32x4 xf, int lane, u32 sb, u32 sq) {
  const int g = lane >> 4, n = lane & 15, qq = n >> 2, p = lane & 3;
  f32x4 acc[4];
#pragma unroll
  for (int t = 0; t < 4; ++t) {
    const u32 a = wl + (8 * g + qq) * 128 + t * 32 + p * 8;
    acc[t] = mfma16(ldstr2(a, a + 512), xf, f32x4{0.f, 0.f, 0.f, 0.f});
  }
  CFENCE();
#pragma unroll
  for (int t = 0; t < 2; ++t) {
    const f32x4 u = acc[t], v = acc[2 + t];
    const bool lo = n < 8;
    *(LAS f32x4*)(wl + (n & 7) * sb + ((n >> 3) * 8 + t * 4 + g) * sq) = f32x4{lo ? u[0] : v[0], lo ? u[1] : v[1], lo ? u[2] : v[2], lo ? u[3] : v[3]};
  }
}
DEV u32x4 qsum(u32 qr, u32 tl, u32 th) {
  f32x4 q[8];
#pragma unroll
  for (int i = 0; i < 8; ++i) q[i] = *(LAS f32x4*)(qr + i * 4096);
  const f32x4 r = ((q[0] + q[1]) + (q[2] + q[3])) + ((q[4] + q[5]) + (q[6] + q[7]));
  return u32x4{pk(r[0], r[1]), pk(r[2], r[3]), tl, th};
}

__global__ __launch_bounds__(512, 2) void gm_kernel(Params P) {
  extern __shared__ __attribute__((aligned(16))) char smem[];
  const int tid = threadIdx.x, lane = tid & 63;
  const int w = __builtin_amdgcn_readfirstlane(tid >> 6);
  const int wg = blockIdx.x, b = wg & 7, idx = (wg >> 3) & 15;
  const int h = HMAP, jq = idx >> 2;
  const rsrc_t ws = __builtin_amdgcn_make_buffer_rsrc(P.ws, 0, WS_BYTES, 0x00020000);
  const int n = lane & 15, g = lane >> 4, qq = n >> 2, p = lane & 3;
  STAMP(0);
  (void)smem;
  if (wg < 128) {
    const int hkv = idx >> 1, half = idx & 1;
    u32x4 ep, xf;
#define QL_CLEAR if (w == 4) { _Pragma("unroll") for (int k = 0; k < 2; ++k) wsst(ws, WS_QL + ((4 * k + g) * 32 + h) * 1024 + jq * 256 + n * 16, u32x4{SENT, SENT, SENT, SENT}); }
    QL_CLEAR
    ldg_sc1(ep, 0u, P.ws);
    const size_t qoff = (size_t)((jq >> 1) * 256 + w * 32) * 8192 + h * 256 + (jq & 1) * 128;
#pragma unroll
    for (int s = 0; s < 4; ++s) dma_nt((u32)((lane >> 3) * 8192 + (lane & 7) * 16), P.wqa + qoff + s * 65536, L_WQA + w * 4096 + s * 1024);
    ldg(xf, (u32)((lane & 7) * 1024 + g * 16), P.x + (jq >> 1) * 512 + w * 64);
    bar();
    if (w < 2) {
#pragma unroll
      for (int k = 0; k < 16; ++k)
        dma_nt((u32)(g * 8192 + ((n ^ k) * 16)), P.wuk + (size_t)(jq * 128 + w * 64 + k * 4) * 8192 + h * 256, L_WUK + w * 16384 + k * 1024);
      waitv<16>();
    } else {
      waitv<0>();
    }
    pin(ep); pin(xf);
    const u64 epoch = (((u64)__builtin_amdgcn_readfirstlane(ep[1]) << 32) | (u32)__builtin_amdgcn_readfirstlane(ep[0])) + 0x0101010101010101ull;
    const u32 tl = (u32)epoch, th = (u32)(epoch >> 32);
    STAMP(1);
    if (tid == 256) wsst(ws, WS_FQ + (h * 4 + jq) * 16, u32x4{0, 0, tl, th});
    qpart(L_WQA + w * 4096, xf, lane, 16, 128);
    bar();
    if ((w >> 1) == 1) wsst(ws, WS_QA + h * 8192 + (2 * (jq & 1) + (w & 1)) * 2048 + (n >> 3) * 1024 + g * 256 + (jq >> 1) * 128 + (lane & 7) * 16, qsum(L_WQA + (w & 1) * 1024 + lane * 16, tl, th));
    STAMP(2);
    const size_t kvoff = ((size_t)(b * 8 + hkv) * 4096 + 3072 + half * 512) * 256;
#define KMAP(j) (w * 4 + (((j) + w + idx + b) & 3))
#define K_ISSUE(R0, CNT, WIN)                                                          \
  _Pragma("nounroll") for (int r = R0; r < R0 + CNT; ++r) {                            \
    waitv<WIN - 1>();                                                                  \
    dma_nt((u32)((g * 256 + (n ^ g) * 16) ^ ((r & 3) * 64)), P.kc + kvoff + KMAP(r >> 2) * 4096 + (r & 3) * 1024, w * 16384 + r * 1024); \
  }
    if (w < 2) {
      u32x4 uk[16], v[8];
      waitv<0>();
      STAMP(10);
#pragma unroll
      for (int t = 0; t < 4; ++t)
#pragma unroll
        for (int s = 0; s < 4; ++s) uk[t * 4 + s] = lds128(L_WUK + w * 16384 + (4 * n + t) * 256 + (((4 * s + g) ^ n) * 16));
      v[0][2] = ~tl;
      POLL(8, v, (v[i][2] ^ tl) | (v[i][3] ^ th), 0, ldg_sc1(v[i], (u32)(lane * 16), P.ws + WS_QA + h * 8192 + i * 1024))
      STAMP(3);
      f32x4 d[4];
#pragma unroll
      for (int t = 0; t < 4; ++t) {
        d[t] = f32x4{0.f, 0.f, 0.f, 0.f};
#pragma unroll
        for (int s = 0; s < 4; ++s) d[t] = mfma16(u32x4{v[2 * s][0], v[2 * s][1], v[2 * s + 1][0], v[2 * s + 1][1]}, uk[t * 4 + s], d[t]);
      }
#pragma unroll
      for (int r = 0; r < 2; ++r) {
        float e[4];
#pragma unroll
        for (int t = 0; t < 4; ++t) {
          auto s2 = __builtin_amdgcn_permlane32_swap(__float_as_uint(d[t][r]), __float_as_uint(d[t][2 + r]), false, false);
          e[t] = (__uint_as_float(s2[0]) + __uint_as_float(s2[1])) * SCL;
        }
        const int bb = 4 * (g & 1) + 2 * (g >> 1) + r;
        wsst2(ws, WS_QL + (bb * 32 + h) * 1024 + jq * 256 + w * 128 + n * 8, u32x2{clampw(pk(e[0], e[1])), clampw(pk(e[2], e[3]))});
      }
    }
    bar();
    STAMP(4);
#pragma unroll
    for (int s = 0; s < 4; ++s) dma_nt((u32)((lane >> 3) * 8192 + (lane & 7) * 16), P.wqb + qoff + s * 65536, L_WQB + w * 4096 + s * 1024);
#pragma unroll
    for (int k = 0; k < 4; ++k) wsst(ws, WS_AP + (h * 4 + jq) * 32768 + (w * 4 + k) * 1024 + lane * 16, u32x4{SENT, SENT, SENT, SENT});
    u32x4 vv[16], pf[2];
    float mx, l;
    f32x4 o[8];
#define V_LOAD ldg_nt(vv[i], (u32)(g * 1024 + n * 16), P.vc + kvoff + KMAP(i >> 2) * 4096 + (i & 3) * 256)
#define V_ISSUE(I0, I1)                                                                                                       \
  _Pragma("unroll") for (int i = I0; i < I1; ++i) {                                                                           \
    waitv<WB - 1>();                                                                                                          \
    V_LOAD;                                                                                                                   \
  }
#define PV_STEP(gm)                                                                                   \
  _Pragma("unroll") for (int k = 0; k < 8; ++k) pin(vv[gm * 8 + k]);                                  \
  _Pragma("unroll") for (int c = 0; c < 4; ++c) {                                                     \
    u32x4 t0, t1;                                                                                     \
    _Pragma("unroll") for (int jp = 0; jp < 4; ++jp) {                                                \
      const u32 a = vv[gm * 8 + 2 * jp][c], bq = vv[gm * 8 + 2 * jp + 1][c];                          \
      t0[jp] = __builtin_amdgcn_perm(bq, a, 0x05040100);                                              \
      t1[jp] = __builtin_amdgcn_perm(bq, a, 0x07060302);                                              \
    }                                                                                                 \
    o[2 * c] = mfma16(pf[gm], t0, gm ? o[2 * c] : f32x4{0.f, 0.f, 0.f, 0.f});                         \
    o[2 * c + 1] = mfma16(pf[gm], t1, gm ? o[2 * c + 1] : f32x4{0.f, 0.f, 0.f, 0.f});                 \
  }
    waitv<0>();
    qpart(L_WQB + w * 4096, xf, lane, 256, 16);
    bar();
    if ((w >> 1) == 1) wsst(ws, WS_QB + ((((w & 1) * 4 + g) * 2 + (jq >> 1)) * 32 + h) * 512 + (jq & 1) * 256 + n * 16, qsum(L_WQB + (w & 1) * 1024 + lane * 16, tl, th));
    if (w == 4 && lane == 0) wsst(ws, WS_FP + (jq * 32 + h) * 16, u32x4{0, 0, tl, th});
    STAMP(5);
    __builtin_amdgcn_s_sleep(BDLY);
    K_ISSUE(0, 16, WB)
    if (w == 0) {
      u32x4 v[4];
      v[0][2] = ~tl;
      POLL(4, v, (v[i][2] ^ tl) | (v[i][3] ^ th), 0, ldg_sc1(v[i], (u32)(lane * 16), P.ws + WS_QB + ((b * 2 + (i >> 1)) * 32 + hkv * 4) * 512 + (i & 1) * 1024))
#pragma unroll
      for (int i = 0; i < 2; ++i)
        *(LAS u32x2*)(L_QBS + i * 512 + lane * 8) = u32x2{pk(lo2f(v[i][0]) + lo2f(v[2 + i][0]), hi2f(v[i][0]) + hi2f(v[2 + i][0])), pk(lo2f(v[i][1]) + lo2f(v[2 + i][1]), hi2f(v[i][1]) + hi2f(v[2 + i][1]))};
    }
    V_ISSUE(0, WB)
      {
        waitv<WB>();
#pragma unroll
        for (int i = WB; i < WB + VX; ++i) V_LOAD;
        STAMP(6);
        bar();
        u32x4 qb[4];
#pragma unroll
        for (int s = 0; s < 4; ++s) qb[s] = lds128(L_QBS + (n & 3) * 256 + s * 64 + g * 16);
        f32x4 sc[4];
        mx = -3.0e38f;
#pragma unroll
        for (int t = 0; t < 4; ++t) {
          sc[t] = f32x4{0.f, 0.f, 0.f, 0.f};
#pragma unroll
          for (int s = 0; s < 4; ++s) sc[t] = mfma16(lds128(w * 16384 + (t * 16 + n) * 256 + (((4 * s + g) ^ n) * 16)), qb[s], sc[t]);
          sc[t] *= SCL;
          mx = fmaxf(fmaxf(mx, fmaxf(sc[t][0], sc[t][1])), fmaxf(sc[t][2], sc[t][3]));
        }
        mx = xor32_max(xor16_max(mx));
        l = 0.f;
#pragma unroll
        for (int t = 0; t < 4; ++t)
#pragma unroll
          for (int e = 0; e < 4; ++e) { sc[t][e] = ex2(sc[t][e] - mx); l += sc[t][e]; }
        l = xor32_sum(xor16_sum(l));
#pragma unroll
        for (int gm = 0; gm < 2; ++gm)
          pf[gm] = u32x4{pk(sc[2 * gm][0], sc[2 * gm][1]), pk(sc[2 * gm][2], sc[2 * gm][3]), pk(sc[2 * gm + 1][0], sc[2 * gm + 1][1]), pk(sc[2 * gm + 1][2], sc[2 * gm + 1][3])};
        if (lane < 4) *(LAS f32x2*)(L_BML + lane * 64 + w * 8) = f32x2{mx, l};
        STAMP(7);
      }
    V_ISSUE(WB + VX, 12)
    bar();
    const int hh = 2 * (w & 1) + (lane >> 5), un = lane & 31;
    float me[8], mlo;
    {
      f32x4 ml[4];
#pragma unroll
      for (int i = 0; i < 4; ++i) ml[i] = *(LAS f32x4*)(L_BML + hh * 64 + i * 16);
      float M = ml[0][0];
#pragma unroll
      for (int i = 1; i < 8; ++i) M = fmaxf(M, ml[i >> 1][(i & 1) * 2]);
      float L = 0.f;
#pragma unroll
      for (int i = 0; i < 8; ++i) {
        me[i] = ex2(ml[i >> 1][(i & 1) * 2] - M);
        L += me[i] * ml[i >> 1][(i & 1) * 2 + 1];
      }
      mlo = (un & 1) ? L : M;
    }
    V_ISSUE(12, 16)
    waitv<8>();
    PV_STEP(0)
    waitv<0>();
    STAMP(8);
    PV_STEP(1)
    if (lane < 16) {
#pragma unroll
      for (int k = 0; k < 4; ++k) {
        *(LAS f32x4*)(L_BM + w * 2048 + k * 512 + n * 32) = f32x4{o[0][k], o[1][k], o[2][k], o[3][k]};
        *(LAS f32x4*)(L_BM + w * 2048 + k * 512 + n * 32 + 16) = f32x4{o[4][k], o[5][k], o[6][k], o[7][k]};
      }
    }
    bar();
    if (w < 2) {
      f32x4 r = {0.f, 0.f, 0.f, 0.f}, r1 = r;
#pragma unroll
      for (int i = 0; i < 4; ++i) {
        r += me[i] * *(LAS f32x4*)(L_BM + i * 2048 + hh * 512 + un * 16);
        r1 += me[4 + i] * *(LAS f32x4*)(L_BM + (4 + i) * 2048 + hh * 512 + un * 16);
      }
      r += r1;
      wsst(ws, WS_BP + (((hkv * 4 + hh) * 2 + half) * 8 + b) * 512 + un * 16, u32x4{pk(r[0], r[1]), pk(r[2], r[3]), tl, __float_as_uint(mlo)});
    }
    STAMP(9);
  } else {
    const char* cb = P.ckv + ((size_t)b * 4096 + idx * 256 + w) * 1024;
#define AROT (2 * b)
#define CROW(i) (cb + (((i) & 16) | (((i) + (AROT)) & 15)) * 8192)
    u32x4 ep, sg[16], v[4], fl[1];
    ldg_sc1(ep, 0u, P.ws);
#define CKV_ISSUE(I0, I1)                                                                                   \
  _Pragma("unroll") for (int i = I0; i < I1; ++i) {                                                         \
    if (i >= WA) waitv<WA - 1>();                                                                           \
    dma_nt((u32)((lane ^ swz(i * 8 + w)) * 16), CROW(i), (u32)((i * 8 + w) * 1024));        \
  }
    __builtin_amdgcn_s_sleep(ADLY);
    CKV_ISSUE(0, WA + 1)
    pin(ep);
    const u64 epoch = (((u64)__builtin_amdgcn_readfirstlane(ep[1]) << 32) | (u32)__builtin_amdgcn_readfirstlane(ep[0])) + 0x0101010101010101ull;
    const u32 tl = (u32)epoch, th = (u32)(epoch >> 32);
    CKV_ISSUE(WA + 1, 16)
#define FQ_LOAD ldg_sc1(fl[0], (u32)(n * 16), P.ws + WS_FQ + w * 256)
    FQ_LOAD;
    bar();
    STAMP(1);
#define QL_LOAD ldg_sc1(v[i], (u32)(lane * 16), P.ws + WS_QL + (b * 32 + 4 * w + i) * 1024)
#define SG_LOAD ldg_nt(sg[i], (u32)((lane ^ swz(i * 8 + w)) * 16), CROW(16 + i))
#pragma unroll
    for (int i = 0; i < NPRE; ++i) { waitv<WA - 1>(); SG_LOAD; }
    waitv<NPRE>();
    POLL(1, fl, (fl[i][2] ^ tl) | (fl[i][3] ^ th), 1, FQ_LOAD)
    STAMP(11);
#pragma unroll
    for (int i = 0; i < 4; ++i) QL_LOAD;
#pragma unroll
    for (int i = NPRE; i < NPRE + NMID; ++i) { waitv<WA + 3>(); SG_LOAD; }
    waitv<NMID>();
    POLL(4, v, (u32)(vmax4(0, v[i]) == SENT), 1, rel_sc1(v[i], vmax4(0, v[i]) == SENT, (u32)(lane * 16), P.ws + WS_QL + (b * 32 + 4 * w + i) * 1024))
    STAMP(2);
#pragma unroll
    for (int i = NPRE + NMID; i < NPRE + NMID + NSTEP; ++i) SG_LOAD;
#pragma unroll
    for (int i = 0; i < 4; ++i) lds128w(L_X + (4 * w + i) * 1024 + ((lane ^ swz(4 * w + i)) * 16), v[i]);
    bar();
    const int r32 = lane & 31, hf = lane >> 5, kg = w & 3, hh2 = w >> 2;
    const u32 fb = n * 1024 + 16 * ((4 * g) ^ swz(n));
    u32x4 qf[16];
#pragma unroll
    for (int ks = 0; ks < 16; ++ks) qf[ks] = lds128(L_X + hh2 * 16384 + (fb ^ (16 * (ks & 3))) + 256 * (ks >> 2));
    f32x16 o[2];
    float m_run = -3.0e38f, m_sc = -3.0e38f, l_run = 0.f;
#pragma unroll
    for (int ph = 0; ph < 2; ++ph) {
      f32x4 sc[2] = {{0.f, 0.f, 0.f, 0.f}, {0.f, 0.f, 0.f, 0.f}};
#pragma unroll
      for (int ks = 0; ks < 16; ++ks)
#pragma unroll
        for (int t = 0; t < 2; ++t) sc[t] = mfma16(lds128(kg * 32768 + t * 16384 + (fb ^ (16 * (ks & 3))) + 256 * (ks >> 2)), qf[ks], sc[t]);
#define AP_OFF(k) (WS_AP + ((((8 * k + (lane >> 3)) * 4 + (w >> 1)) * 8 + b) * 16 + idx) * 256 + (w & 1) * 128 + (lane & 7) * 16)
      if (ph == 0) {
#pragma unroll
        for (int i = NPRE + NMID + NSTEP; i < NPRE + NMID + 2 * NSTEP && i < 16; ++i) SG_LOAD;
      }
      if (ph == 1 && WOAP == 0) {
#pragma unroll
        for (int i = 4; i < 8; ++i) ldg_nt(sg[i], (u32)(g * 1024 + n * 16), P.woa + (size_t)(h * 128 + (4 * w + (i & 3)) * 4) * 1024 + jq * 256);
      }
      float mx = fmaxf(fmaxf(fmaxf(sc[0][0], sc[0][1]), fmaxf(sc[0][2], sc[0][3])), fmaxf(fmaxf(sc[1][0], sc[1][1]), fmaxf(sc[1][2], sc[1][3])));
      mx = xor32_max(xor16_max(mx));
      if (ph == 0) bar();
      if (lane < 16) *(LAS float*)(L_MAXB + (hh2 * 16 + n) * 16 + kg * 4) = mx;
      bar();
      const f32x4 ma = *(LAS f32x4*)(L_MAXB + (hh2 * 16 + n) * 16), mb = *(LAS f32x4*)(L_MAXB + r32 * 16);
      mx = fmaxf(fmaxf(fmaxf(ma[0], ma[1]), fmaxf(ma[2], ma[3])), m_sc);
      float l = 0.f;
#pragma unroll
      for (int t = 0; t < 2; ++t) {
#pragma unroll
        for (int i = 0; i < 4; ++i) { sc[t][i] = ex2(sc[t][i] - mx); l += sc[t][i]; }
        *(LAS u32x2*)(L_P + (2 * kg + t) * 1024 + ((g & 1) * 32 + hh2 * 16 + n) * 16 + (g >> 1) * 8) = u32x2{pk(sc[t][0], sc[t][1]), pk(sc[t][2], sc[t][3])};
      }
      l_run = l_run * ex2(m_sc - mx) + l;
      m_sc = mx;
      if (ph == 1) {
        const float ls = xor32_sum(xor16_sum(l_run));
        if (lane < 16) *(LAS float*)(L_SUMB + (hh2 * 16 + n) * 16 + kg * 4) = ls;
      }
      const float mo = fmaxf(fmaxf(fmaxf(mb[0], mb[1]), fmaxf(mb[2], mb[3])), m_run);
      const float alpha = ex2(m_run - mo);
      m_run = mo;
#pragma unroll
      for (int ct = 0; ct < 2; ++ct)
#pragma unroll
        for (int i = 0; i < 16; ++i) o[ct][i] = ph ? o[ct][i] * alpha : 0.f;
      bar();
      STAMP(3 + 3 * ph);
      if (ph == 0) {
#pragma unroll
        for (int i = NPRE + NMID + 2 * NSTEP; i < NPRE + NMID + 3 * NSTEP && i < 16; ++i) SG_LOAD;
      }
      if (ph == 1 && WOAP == 1) {
#pragma unroll
        for (int i = 4; i < 8; ++i) ldg_nt(sg[i], (u32)(g * 1024 + n * 16), P.woa + (size_t)(h * 128 + (4 * w + (i & 3)) * 4) * 1024 + jq * 256);
      }
      if (ph == 1 && w == 0) {
        const f32x4 a = *(LAS f32x4*)(L_SUMB + r32 * 16);
        const float lsum = (a[0] + a[1]) + (a[2] + a[3]);
        if (lane < 32) wsst(ws, WS_ML + ((b * 16 + idx) * 32 + lane) * 16, u32x4{__float_as_uint(m_run), __float_as_uint(lsum), tl, th});
      }
      {
        const int blk = g & 1;
        const u32 a0 = ((4 * hf + qq) * 1024 + (w >> 1) * 256 + 16 * ((2 * blk + (p >> 1)) ^ ((qq << 2) | hf)) + 8 * (p & 1)) ^ (128 * (w & 1));
        const u32 a1 = (a0 ^ 32) + 8192;
#pragma unroll
        for (int ks = 0; ks < 8; ++ks) {
          if (ph == 0 && ks == 4) {
#pragma unroll
            for (int i = NPRE + NMID + 3 * NSTEP; i < 16; ++i) SG_LOAD;
          }
          const u32x4 pf = lds128(L_P + ks * 1024 + lane * 16);
#pragma unroll
          for (int ct = 0; ct < 2; ++ct) o[ct] = mfma32(ldstr2((a0 ^ (64 * ct)) + 16384 * ks, (a1 ^ (64 * ct)) + 16384 * ks), pf, o[ct]);
        }
      }
      if (ph == 0) {
        bar();
        STAMP(4);
        waitv<0>();
#pragma unroll
        for (int i = 0; i < 16; ++i) { pin(sg[i]); lds128w((i * 8 + w) * 1024 + lane * 16, sg[i]); }
#define FP_LOAD ldg_sc1(fl[0], (u32)(r32 * 16), P.ws + WS_FP + (w >> 1) * 512)
        waitl();
        FP_LOAD;
        ldg(sg[8], (u32)(lane * 16), P.gw);
        ldg(sg[9], (u32)(lane * 16), P.x + w * 1024);
#pragma unroll
        for (int i = 0; i < 4; ++i) ldg_nt(sg[i], (u32)(g * 8192 + n * 16), P.wuv + (size_t)(jq * 128 + (4 * w + i) * 4) * 8192 + h * 256);
        bar();
        STAMP(5);
      }
    }
    STAMP(7);
#define AP_ROW(r) ((r) * 1024 + (w >> 1) * 256 + (((w ^ ((r) >> 1)) & 1) * 128))
    CFENCE();
#pragma unroll
    for (int ct = 0; ct < 2; ++ct)
#pragma unroll
      for (int uu = 0; uu < 2; ++uu)
        lds128w(AP_ROW(r32) + (((ct * 4 + hf * 2 + uu) ^ (r32 & 7)) * 16),
                clamp4(u32x4{pk(o[ct][8 * uu], o[ct][8 * uu + 1]), pk(o[ct][8 * uu + 2], o[ct][8 * uu + 3]), pk(o[ct][8 * uu + 4], o[ct][8 * uu + 5]), pk(o[ct][8 * uu + 6], o[ct][8 * uu + 7])}));
    waitv<8>();
    POLL(1, fl, (fl[i][2] ^ tl) | (fl[i][3] ^ th), 1, FP_LOAD)
    waitl();
#pragma unroll
    for (int k = 0; k < 4; ++k) {
      const int hh = 8 * k + (lane >> 3);
      wsst(ws, AP_OFF(k), lds128(AP_ROW(hh) + (((lane & 7) ^ (hh & 7)) * 16)));
    }
    STAMP(8);
    u32x4 wu[4], wa[4], wb[4];
    float gate;
    {
      u32x4 mlv[1];
      ldg_sc1(mlv[0], (u32)(n * 512), P.ws + WS_ML + (w * 512 + h) * 16);
      bar();
      waitv<5>();
#pragma unroll
      for (int i = 0; i < 8; ++i) { pin(sg[i]); lds128w((i >> 2) * 32768 + (4 * w + (i & 3)) * 1024 + lane * 16, sg[i]); }
      bar();
      waitv<0>();
      POLL(1, mlv, (mlv[i][2] ^ tl) | (mlv[i][3] ^ th), 2, ldg_sc1(mlv[i], (u32)(n * 512), P.ws + WS_ML + (w * 512 + h) * 16))
      const u32x4 ml = mlv[0];
#pragma unroll
      for (int i = 0; i < 4; ++i) ldg_sc1(v[i], (u32)(lane * 16), P.ws + WS_AP + ((h * 4 + jq) * 8 + w) * 4096 + i * 1024);
#pragma unroll
      for (int i = 0; i < 4; ++i) {
        const u32 a = L_WUV + (32 * i + 16 * (g & 1) + 4 * (g >> 1) + qq) * 256 + w * 32 + p * 8;
        wu[i] = ldstr2(a, a + 2048);
        const u32 a2 = L_WOA + (32 * i + 8 * g + qq) * 256 + w * 32 + p * 8;
        wa[i] = ldstr2(a2, a2 + 1024);

      }
      pin(sg[8]); pin(sg[9]);
    {
      float a = 0.f;
#pragma unroll
      for (int e = 0; e < 4; ++e) a += rbf(lo2f(sg[9][e]) * lo2f(sg[8][e])) + rbf(hi2f(sg[9][e]) * hi2f(sg[8][e]));
      a += dpp<ROR1>(a); a += dpp<ROR2>(a); a += dpp<ROR4>(a); a += dpp<ROR8>(a);
      a = xor32_sum(xor16_sum(a));
      gate = rbf(1.f / (1.f + __expf(-rbf(a))));
    }
      if (lane == 0) *(LAS float*)(L_CL + 4096 + w * 4) = gate;
      const float ms = __uint_as_float(ml[0]);
      float M = ms;
      M = fmaxf(M, dpp<ROR1>(M)); M = fmaxf(M, dpp<ROR2>(M)); M = fmaxf(M, dpp<ROR4>(M)); M = fmaxf(M, dpp<ROR8>(M));
      const float e = ex2(ms - M);
      float L = e * __uint_as_float(ml[1]);
      L += dpp<ROR1>(L); L += dpp<ROR2>(L); L += dpp<ROR4>(L); L += dpp<ROR8>(L);
      const float wgt = e * __builtin_amdgcn_rcpf(L);
      float wv[4], acc[8];
#pragma unroll
      for (int i = 0; i < 4; ++i) wv[i] = __uint_as_float(__builtin_amdgcn_ds_bpermute((4 * i + g) * 4, __float_as_uint(wgt)));
      waitv<0>();
      POLL(4, v, (u32)(vmax4(0, v[i]) == SENT), 1, rel_sc1(v[i], vmax4(0, v[i]) == SENT, (u32)(lane * 16), P.ws + WS_AP + ((h * 4 + jq) * 8 + w) * 4096 + i * 1024))
      STAMP(9);
#pragma unroll
      for (int i = 0; i < 4; ++i) dma_nt((u32)(g * 1024 + n * 16), P.wob + (size_t)(h * 128 + (4 * w + i) * 4) * 1024 + jq * 256, L_WOB + (4 * w + i) * 1024);
#pragma unroll
      for (int i = 0; i < 4; ++i) {
#pragma unroll
        for (int e2 = 0; e2 < 4; ++e2) {
          acc[2 * e2] = (i ? acc[2 * e2] : 0.f) + wv[i] * lo2f(v[i][e2]);
          acc[2 * e2 + 1] = (i ? acc[2 * e2 + 1] : 0.f) + wv[i] * hi2f(v[i][e2]);
        }
      }
      float r4[4], r2[2];
#pragma unroll
      for (int e2 = 0; e2 < 4; ++e2) {
        auto r = __builtin_amdgcn_permlane32_swap(__float_as_uint(acc[e2]), __float_as_uint(acc[4 + e2]), false, false);
        r4[e2] = __uint_as_float(r[0]) + __uint_as_float(r[1]);
      }
#pragma unroll
      for (int e2 = 0; e2 < 2; ++e2) {
        auto r = __builtin_amdgcn_permlane16_swap(__float_as_uint(r4[e2]), __float_as_uint(r4[2 + e2]), false, false);
        r2[e2] = __uint_as_float(r[0]) + __uint_as_float(r[1]);
      }
      *(LAS u32*)(L_CL + (n * 8 + w) * 16 + g * 4) = pk(r2[0], r2[1]);
    }
    bar();
    STAMP(10);
    {
      f32x4 d = {0.f, 0.f, 0.f, 0.f};
#pragma unroll
      for (int ks = 0; ks < 4; ++ks) {
        d = mfma16(wu[ks], lds128(L_CL + ((ks * 4 + g) * 8 + (n & 7)) * 16), d);
      }
      if (n < 8) wsst(ws, WS_CT + (h * 4 + jq) * 4096 + (w * 4 + g) * 128 + n * 16, u32x4{pk(d[0], d[1]), pk(d[2], d[3]), tl, th});
    }
    {
      const f32x4 gg = *(LAS f32x4*)(L_CL + 4096 + (g & 1) * 16);
#define CB_ADDR (u32)(hf * 4096 + r32 * 16), P.ws + (u32)__builtin_amdgcn_readfirstlane((i < 2 ? WS_CT + (h * 4 + 2 * i) * 4096 : WS_BP + h * 8192) + w * 512)
#define CB_BAD ((v[i][2] ^ tl) | (i < 2 ? v[i][3] ^ th : 0u))
      waitv<0>();
#pragma unroll
      for (int i = 0; i < 3; ++i) ldg_sc1(v[i], CB_ADDR);
      bar();
#pragma unroll
      for (int i = 0; i < 4; ++i) {
        const u32 a = L_WOB + (32 * i + 8 * g + qq) * 256 + w * 32 + p * 8;
        wb[i] = ldstr2(a, a + 1024);
      }
      waitv<0>();
      POLL(3, v, CB_BAD, 0, rel_sc1(v[i], CB_BAD != 0, CB_ADDR))
      STAMP(12);
      {
        auto s0 = __builtin_amdgcn_permlane32_swap(__float_as_uint(lo2f(v[0][0]) + lo2f(v[1][0])), __float_as_uint(lo2f(v[0][1]) + lo2f(v[1][1])), false, false);
        auto s1 = __builtin_amdgcn_permlane32_swap(__float_as_uint(hi2f(v[0][0]) + hi2f(v[1][0])), __float_as_uint(hi2f(v[0][1]) + hi2f(v[1][1])), false, false);
        *(LAS u32*)(L_CL + 8192 + ((2 * w + (r32 >> 4)) * 8 + (r32 & 7)) * 16 + ((r32 >> 3) & 1) * 8 + hf * 4) =
            pk(__uint_as_float(s0[0]) + __uint_as_float(s0[1]), __uint_as_float(s1[0]) + __uint_as_float(s1[1]));
      }
      {
        const float x = __uint_as_float(v[2][3]), y = dpp<0x141>(x);
        const float m0 = (lane & 1) ? y : x, l0 = (lane & 1) ? x : y;
        const float e = ex2(m0 - xor32_max(m0));
        const float wgt = e * rbf(1.f - gate) * __builtin_amdgcn_rcpf(xor32_sum(e * l0));
        auto s0 = __builtin_amdgcn_permlane32_swap(__float_as_uint(wgt * lo2f(v[2][0])), __float_as_uint(wgt * lo2f(v[2][1])), false, false);
        auto s1 = __builtin_amdgcn_permlane32_swap(__float_as_uint(wgt * hi2f(v[2][0])), __float_as_uint(wgt * hi2f(v[2][1])), false, false);
        *(LAS u32*)(L_CL + 12288 + ((r32 >> 1) * 8 + w) * 16 + (r32 & 1) * 8 + hf * 4) =
            pk(__uint_as_float(s0[0]) + __uint_as_float(s0[1]), __uint_as_float(s1[0]) + __uint_as_float(s1[1]));
      }
      bar();
      STAMP(13);
      f32x4 da = {0.f, 0.f, 0.f, 0.f}, db = {0.f, 0.f, 0.f, 0.f};
#pragma unroll
      for (int i = 0; i < 4; ++i) {
        da = mfma16(lds128(L_CL + 8192 + ((4 * i + g) * 8 + (n & 7)) * 16), wa[i], da);
        db = mfma16(lds128(L_CL + 12288 + ((4 * i + g) * 8 + (n & 7)) * 16), wb[i], db);
      }
      da = da * gg + db;
      u32x4 ov;
#pragma unroll
      for (int r = 0; r < 4; ++r) ov[r] = (__float_as_uint(da[r]) & 0xFFFFFF00u) | ((tl >> (8 * r)) & 0xFFu);
      if (lane < 32) wsst(ws, WS_OP + ((jq * 32 + w * 4 + (lane >> 3)) * 32 + h) * 128 + (lane & 7) * 16, ov);
      STAMP(14);
      if (w == 0) {
        const int red = wg - 128;
        waitv<0>();
        v[0][0] = ~tl;
        POLL(4, v, __builtin_amdgcn_perm(__builtin_amdgcn_perm(v[i][3], v[i][2], 0x0c0c0400), __builtin_amdgcn_perm(v[i][1], v[i][0], 0x0c0c0400), 0x05040100) ^ tl, 0,
             ldg_sc1(v[i], (u32)(lane * 16), P.ws + WS_OP + red * 4096 + i * 1024))
        float r[4];
#pragma unroll
        for (int e2 = 0; e2 < 4; ++e2) {
          float x = (__uint_as_float(v[0][e2] & 0xFFFFFF00u) + __uint_as_float(v[1][e2] & 0xFFFFFF00u)) + (__uint_as_float(v[2][e2] & 0xFFFFFF00u) + __uint_as_float(v[3][e2] & 0xFFFFFF00u));
          x += dpp<ROR8>(x);
          r[e2] = xor32_sum(xor16_sum(x));
        }
        const float rv = g == 0 ? r[0] : g == 1 ? r[1] : g == 2 ? r[2] : r[3];
        const int L = red & 7, col = (red >> 3) * 32 + 16 * (L >> 2) + 8 * (L & 1) + n, bb = 4 * ((L >> 1) & 1) + g;
        if (n < 8) *(unsigned short*)(P.out + (bb * 512 + col) * 2) = (unsigned short)pk(rv, 0.f);
        if (wg == 128 && lane == 0) wsst2(ws, WS_EPOCH, u32x2{tl, th});
        STAMP(15);
      }
    }
  }
}

extern "C" void kernel_launch(void* const* d_in, const int* in_sizes, int n_in, void* d_out, int out_size, void* d_ws, size_t ws_size, hipStream_t stream) {
  Params p;
  p.x = (const char*)d_in[0]; p.ckv = (const char*)d_in[1]; p.wqa = (const char*)d_in[2]; p.wuk = (const char*)d_in[3];
  p.wuv = (const char*)d_in[4]; p.woa = (const char*)d_in[5]; p.kc = (const char*)d_in[7]; p.vc = (const char*)d_in[8];
  p.wqb = (const char*)d_in[9]; p.wob = (const char*)d_in[10]; p.gw = (const char*)d_in[11];
  p.out = (char*)d_out; p.ws = (char*)d_ws;
  (void)hipFuncSetAttribute((const void*)gm_kernel, hipFuncAttributeMaxDynamicSharedMemorySize, LDS_BYTES);
  hipLaunchKernelGGL(gm_kernel, dim3(256), dim3(512), LDS_BYTES, stream, p);
}
```
